```python
import torch

def _softmax(x, axis=-1):
    return torch.softmax(x, dim=axis)

def _gelu(x):
    return torch.nn.functional.gelu(x, approximate='tanh')

def _silu(x):
    return torch.nn.functional.silu(x)

def _sigmoid(x):
    return torch.sigmoid(x)

def _layernorm(x, axis=-1, eps=1e-05):
    m = x.mean(dim=axis, keepdim=True)
    v = x.var(dim=axis, keepdim=True, unbiased=False)
    return (x - m) / torch.sqrt(v + eps)

def _scatter_add(tgt, idx, src, axis=0):
    return tgt.clone().index_add_(axis, idx, src)

def _segment_sum(src, seg, n):
    out = torch.zeros((n,) + src.shape[1:], dtype=src.dtype)
    return out.index_add_(0, seg, src)

def _pad(x, pw):
    flat = tuple((v for pair in reversed(pw) for v in pair))
    return torch.nn.functional.pad(x, flat)
kernel_name = 'adaLN_cond__sliding_block_cached__swiglu_ffn__decode'

def setup_inputs():

    _w = lambda a: (lambda t: t.to(torch.bfloat16) if t.is_floating_point() else t)(torch.as_tensor(a))

    def _A_setup():
        B, Sq, D = (8, 1, 512)
        Hq, Hkv, Sk, Dh = (32, 8, 8192, 128)
        assert Hq % Hkv == 0, (Hq, Hkv)
        return {'x': _w((lambda *s: torch.randn(*s, dtype=torch.bfloat16) * 0.05)(B, Sq, D)), 'k_cache': _w((lambda *s: torch.randn(*s, dtype=torch.bfloat16) * 3.2)(B, Hkv, Sk, Dh)), 'v_cache': _w((lambda *s: torch.randn(*s, dtype=torch.bfloat16) * 0.4)(B, Hkv, Sk, Dh)), 'wq': _w((lambda *s: torch.randn(*s, dtype=torch.bfloat16) * 3.2)(D, Hq * Dh) * D ** (-0.5)), 'wo': _w((lambda *s: torch.randn(*s, dtype=torch.bfloat16) * 0.4)(Hq * Dh, D) * (Hq * Dh) ** (-0.5))}
    _A_in = _A_setup()

    def _B_setup():
        B, S, D, F = (8, 1, 512, 1792)
        return {'x': _w((lambda *s: torch.randn(*s, dtype=torch.bfloat16) * 0.05)(B, S, D)), 'w_gate': _w((lambda *s: torch.randn(*s, dtype=torch.bfloat16) * 0.05)(D, F)), 'w_up': _w((lambda *s: torch.randn(*s, dtype=torch.bfloat16) * 0.05)(D, F)), 'w_down': _w((lambda *s: torch.randn(*s, dtype=torch.bfloat16) * 0.05)(F, D))}
    _B_in = _B_setup()
    return {'A_x': _A_in['x'], 'A_k_cache': _A_in['k_cache'], 'A_v_cache': _A_in['v_cache'], 'A_wq': _A_in['wq'], 'A_wo': _A_in['wo'], 'B_x': _B_in['x'], 'B_w_gate': _B_in['w_gate'], 'B_w_up': _B_in['w_up'], 'B_w_down': _B_in['w_down'], 'c': _w((lambda *s: torch.randn(*s, dtype=torch.bfloat16) * 0.1)(8, 512) * 8.0), 'w_mod': _w((lambda *s: torch.randn(*s, dtype=torch.bfloat16) * 0.1)(512, 6 * 512) * 512 ** (-0.5) * 8.0)}

def reference(A_x, A_k_cache, A_v_cache, A_wq, A_wo, B_x, B_w_gate, B_w_up, B_w_down, c, w_mod):

    def _A_ref(x, k_cache, v_cache, wq, wo):
        B, Sq, D = x.shape
        Hq, Dh = (32, 128)
        Hkv, Sk = (k_cache.shape[1], k_cache.shape[2])
        W = min(4096, max(256, Sk // 4))
        rep = Hq // Hkv
        q = (lambda x, *d: x.permute(*d))((x @ wq).reshape(B, Sq, Hq, Dh), 0, 2, 1, 3)
        k = (lambda x, n, axis: torch.repeat_interleave(x, n, dim=axis))(k_cache[:, :, Sk - W:, :], rep, axis=1)
        v = (lambda x, n, axis: torch.repeat_interleave(x, n, dim=axis))(v_cache[:, :, Sk - W:, :], rep, axis=1)
        scores = torch.einsum('bhqd,bhkd->bhqk', q, k) * Dh ** (-0.5)
        p = _softmax(scores, axis=-1)
        o = torch.einsum('bhqk,bhkd->bhqd', p, v)
        return (lambda x, *d: x.permute(*d))(o, 0, 2, 1, 3).reshape(B, Sq, Hq * Dh) @ wo

    def _B_ref(x, w_gate, w_up, w_down):
        g = _silu(x @ w_gate)
        u = x @ w_up
        return g * u @ w_down
    x = A_x
    mod = (_silu(c) @ w_mod).reshape(c.shape[0], 6, 1, -1)
    sa, ca, ga, sb, cb, gb = (mod[:, 0].clone(), mod[:, 1].clone(), mod[:, 2].clone(), mod[:, 3].clone(), mod[:, 4].clone(), mod[:, 5].clone())
    h = _layernorm(x, axis=-1) * (1 + ca) + sa
    x = x + ga * _A_ref(h, A_k_cache, A_v_cache, A_wq, A_wo)
    h = _layernorm(x, axis=-1) * (1 + cb) + sb
    x = x + gb * _B_ref(h, B_w_gate, B_w_up, B_w_down)
    return x
```

```cpp
#include <hip/hip_runtime.h>
#include <hip/hip_bf16.h>
#include <stdint.h>

#define EMU 0

typedef unsigned short u16;
__device__ __forceinline__ float bf2f(u16 b) { return __uint_as_float(((unsigned)b) << 16); }
__device__ __forceinline__ u16 f2bf(float f) { unsigned u = __float_as_uint(f); u += 0x7FFFu + ((u >> 16) & 1u); return (u16)(u >> 16); }
__device__ __forceinline__ float rb(float f) {
  return f;
}

constexpr int B = 8, D = 512, HQ = 32, HKV = 8, SK = 8192, DH = 128, W = 2048, F = 1792;

__global__ void gemv8(const float* __restrict__ x, const u16* __restrict__ w, float* __restrict__ out, int K, int N) {
  __shared__ float red[4][8][64];
  int n = blockIdx.x * 64 + (threadIdx.x & 63);
  int wv = threadIdx.x >> 6;
  float acc[8];
  for (int b = 0; b < 8; ++b) acc[b] = 0.f;
  for (int k = wv; k < K; k += 4) {
    float wvv = bf2f(w[(size_t)k * N + n]);
    for (int b = 0; b < 8; ++b) acc[b] += x[b * K + k] * wvv;
  }
  for (int b = 0; b < 8; ++b) red[wv][b][threadIdx.x & 63] = acc[b];
  __syncthreads();
  if (wv == 0) for (int b = 0; b < 8; ++b) {
    int l = threadIdx.x;
    out[b * N + n] = rb(red[0][b][l] + red[1][b][l] + red[2][b][l] + red[3][b][l]);
  }
}

__global__ void silu_c(const u16* c, float* sc) {
  int i = blockIdx.x * 256 + threadIdx.x;
  float v = bf2f(c[i]);
  sc[i] = rb(v / (1.f + __expf(-v)));
}

__global__ void ln_mod(const float* x, const float* mod, int s_off, int c_off, float* h) {
  __shared__ float red[512];
  int b = blockIdx.x, t = threadIdx.x;
  float v = x[b * D + t];
  red[t] = v; __syncthreads();
  for (int s = 256; s > 0; s >>= 1) { if (t < s) red[t] += red[t + s]; __syncthreads(); }
  float mean = red[0] / D; __syncthreads();
  float dd = v - mean;
  red[t] = dd * dd; __syncthreads();
  for (int s = 256; s > 0; s >>= 1) { if (t < s) red[t] += red[t + s]; __syncthreads(); }
  float var = red[0] / D;
  float m = rb(mean), vv = rb(var);
  float num = rb(v - m);
  float den = rb(sqrtf(rb(vv + 1e-5f)));
  float n = rb(num / den);
  float cm = rb(1.f + mod[b * 6 * D + c_off + t]);
  h[b * D + t] = rb(rb(n * cm) + mod[b * 6 * D + s_off + t]);
}

__global__ void cvt_x(const u16* x, float* xf) { int i = blockIdx.x * 256 + threadIdx.x; xf[i] = bf2f(x[i]); }

__global__ void attn(const float* q, const u16* kc, const u16* vc, float* o) {
  __shared__ float s[W];
  __shared__ float red[256];
  __shared__ float qs[DH];
  int b = blockIdx.x / HQ, h = blockIdx.x % HQ, kvh = h / (HQ / HKV), t = threadIdx.x;
  if (t < DH) qs[t] = q[b * HQ * DH + h * DH + t];
  __syncthreads();
  const u16* kb = kc + ((size_t)(b * HKV + kvh) * SK + (SK - W)) * DH;
  const u16* vb = vc + ((size_t)(b * HKV + kvh) * SK + (SK - W)) * DH;
  float mx = -INFINITY;
  for (int k = t; k < W; k += 256) {
    float a = 0.f;
    for (int d = 0; d < DH; ++d) a += qs[d] * bf2f(kb[(size_t)k * DH + d]);
    a = rb(rb(a) * 0.08838834764831845f);
    s[k] = a; mx = fmaxf(mx, a);
  }
  red[t] = mx; __syncthreads();
  for (int st = 128; st > 0; st >>= 1) { if (t < st) red[t] = fmaxf(red[t], red[t + st]); __syncthreads(); }
  mx = red[0]; __syncthreads();
  float sum = 0.f;
  for (int k = t; k < W; k += 256) { float e = __expf(s[k] - mx); s[k] = e; sum += e; }
  red[t] = sum; __syncthreads();
  for (int st = 128; st > 0; st >>= 1) { if (t < st) red[t] += red[t + st]; __syncthreads(); }
  sum = red[0]; __syncthreads();
  float inv = 1.f / sum;
  int d = t & 127, half = t >> 7;
  float acc = 0.f;
  for (int k = half; k < W; k += 2) acc += rb(s[k] * inv) * bf2f(vb[(size_t)k * DH + d]);
  red[t] = acc; __syncthreads();
  if (t < 128) o[b * HQ * DH + h * DH + t] = rb(red[t] + red[t + 128]);
}

__global__ void resid(const float* x, const float* mod, int g_off, const float* a, float* x2, u16* outb) {
  int i = blockIdx.x * 256 + threadIdx.x; int b = i / D, t = i % D;
  float v = rb(x[i] + rb(mod[b * 6 * D + g_off + t] * a[i]));
  if (x2) x2[i] = v;
  if (outb) outb[i] = f2bf(v);
}
__global__ void swi(const float* g, const float* u, float* act) {
  int i = blockIdx.x * 256 + threadIdx.x;
  float v = g[i];
  act[i] = rb(rb(v / (1.f + __expf(-v))) * u[i]);
}

extern "C" void kernel_launch(void* const* d_in, const int* in_sizes, int n_in, void* d_out, int out_size, void* d_ws, size_t ws_size, hipStream_t stream) {
  const u16* x = (const u16*)d_in[0]; const u16* kc = (const u16*)d_in[1]; const u16* vc = (const u16*)d_in[2];
  const u16* wq = (const u16*)d_in[3]; const u16* wo = (const u16*)d_in[4];
  const u16* wg = (const u16*)d_in[6]; const u16* wu = (const u16*)d_in[7]; const u16* wd = (const u16*)d_in[8];
  const u16* c = (const u16*)d_in[9]; const u16* wm = (const u16*)d_in[10];
  float* ws = (float*)d_ws;
  float* sc = ws; float* mod = sc + 4096; float* xf = mod + 8 * 3072; float* h = xf + 4096; float* q = h + 4096;
  float* o = q + 8 * 4096; float* a = o + 8 * 4096; float* x2 = a + 4096; float* h2 = x2 + 4096; float* g = h2 + 4096; float* u = g + 8 * F; float* act = u + 8 * F; float* f = act + 8 * F;
  silu_c<<<16, 256, 0, stream>>>(c, sc);
  cvt_x<<<16, 256, 0, stream>>>(x, xf);
  gemv8<<<3072 / 64, 256, 0, stream>>>(sc, wm, mod, 512, 3072);
  ln_mod<<<8, 512, 0, stream>>>(xf, mod, 0, 512, h);
  gemv8<<<4096 / 64, 256, 0, stream>>>(h, wq, q, 512, 4096);
  attn<<<B * HQ, 256, 0, stream>>>(q, kc, vc, o);
  gemv8<<<512 / 64, 256, 0, stream>>>(o, wo, a, 4096, 512);
  resid<<<16, 256, 0, stream>>>(xf, mod, 1024, a, x2, nullptr);
  ln_mod<<<8, 512, 0, stream>>>(x2, mod, 1536, 2048, h2);
  gemv8<<<F / 64, 256, 0, stream>>>(h2, wg, g, 512, F);
  gemv8<<<F / 64, 256, 0, stream>>>(h2, wu, u, 512, F);
  swi<<<8 * F / 256, 256, 0, stream>>>(g, u, act);
  gemv8<<<512 / 64, 256, 0, stream>>>(act, wd, f, F, 512);
  resid<<<16, 256, 0, stream>>>(x2, mod, 2560, f, nullptr, (u16*)d_out);
}
```

```cpp
#define PFC 7
#include <hip/hip_runtime.h>
#include <hip/hip_bf16.h>
#include <stdint.h>

#define STAMPS 0
#define DEPTH 6
#define T_PF 0
#define ABL 0
#define PREW 1
#define CODEPF 20

namespace {

typedef unsigned v4u __attribute__((ext_vector_type(4)));
typedef unsigned v2u __attribute__((ext_vector_type(2)));
typedef float f32x4 __attribute__((ext_vector_type(4)));
typedef short s16x4 __attribute__((ext_vector_type(4)));
typedef __bf16 bf16x8 __attribute__((ext_vector_type(8)));
typedef __bf16 bf16x2 __attribute__((ext_vector_type(2)));
typedef unsigned long long u64;
typedef __amdgpu_buffer_rsrc_t rsrc_t;
#define LAS __attribute__((address_space(3)))
typedef LAS unsigned char* lptr;

constexpr int SK = 8192, WIN = 2048, FF = 1792;
constexpr int SC1 = 16, NT = 2;
constexpr int WS_EPOCH = 0;
constexpr int WS_MC = 4096;
constexpr int WS_MP = WS_MC + 131072;
constexpr int WS_MOD = WS_MP + 262144;
constexpr int WS_QP = WS_MOD + 65536;
constexpr int WS_OP = WS_QP + 524288;
constexpr int WS_ST = WS_OP + 524288;
constexpr int WS_AP = WS_ST + 16384;
constexpr int WS_FP = WS_AP + 131072;
constexpr int WS_TOTAL = WS_FP + 458752;
constexpr int WS_STAMP = 4 << 20;
constexpr int L_EM = 98304, L_EQ = 114688;
constexpr int L_FLAGS = 131072, L_QS = L_FLAGS + 256, L_MST = L_QS + 1024, L_STAT = L_MST + 128, L_PB = L_STAT + 128, L_OB = L_PB + 4096, L_RED = L_OB + 16384,
              L_TOTAL = L_RED + 8192;
static_assert(L_TOTAL <= 163840, "LDS");
enum { F_EW = 0, F_M = 1, F_LN = 2, F_Q = 3, F_QR = 4, F_L = 5, F_VI = 6, F_V = 7, F_PUB = 8, F_TAG = 9, F_KI = 10, F_N = 16 };
constexpr unsigned GSPIN = 1u << 16, LSPIN = 1u << 20;
constexpr float LOG2E = 1.4426950408889634f;

__device__ __forceinline__ float bflo(unsigned u) { return __uint_as_float(u << 16); }
__device__ __forceinline__ float bfhi(unsigned u) { return __uint_as_float(u & 0xffff0000u); }
__device__ __forceinline__ unsigned pk2(float a, float b) { bf16x2 p = {(__bf16)a, (__bf16)b}; return __builtin_bit_cast(unsigned, p); }
__device__ __forceinline__ float rb(float a) { unsigned u = pk2(a, a); asm("" : "+v"(u)); return bfhi(u); }
template <int CTRL> __device__ __forceinline__ float dpp(float x) { return __builtin_bit_cast(float, __builtin_amdgcn_mov_dpp(__builtin_bit_cast(int, x), CTRL, 0xf, 0xf, true)); }
constexpr int XOR1 = 0xB1, XOR2 = 0x4E, HMIR = 0x141, ROR8 = 0x128, ROR4 = 0x124;
__device__ __forceinline__ float x16_sum(float x) { auto s = __builtin_amdgcn_permlane16_swap(__float_as_uint(x), __float_as_uint(x), false, false); return __uint_as_float(s[0]) + __uint_as_float(s[1]); }
__device__ __forceinline__ float x32_sum(float x) { auto s = __builtin_amdgcn_permlane32_swap(__float_as_uint(x), __float_as_uint(x), false, false); return __uint_as_float(s[0]) + __uint_as_float(s[1]); }
__device__ __forceinline__ float x16_max(float x) { auto s = __builtin_amdgcn_permlane16_swap(__float_as_uint(x), __float_as_uint(x), false, false); return fmaxf(__uint_as_float(s[0]), __uint_as_float(s[1])); }
__device__ __forceinline__ float x32_max(float x) { auto s = __builtin_amdgcn_permlane32_swap(__float_as_uint(x), __float_as_uint(x), false, false); return fmaxf(__uint_as_float(s[0]), __uint_as_float(s[1])); }
__device__ __forceinline__ float half_sum(float x) { x += dpp<XOR1>(x); x += dpp<XOR2>(x); x += dpp<HMIR>(x); x += dpp<ROR8>(x); return x16_sum(x); }
__device__ __forceinline__ float wave_sum(float x) { return x32_sum(half_sum(x)); }
__device__ __forceinline__ f32x4 mfma(v4u a, v4u b, f32x4 c) { return __builtin_amdgcn_mfma_f32_16x16x32_bf16(__builtin_bit_cast(bf16x8, a), __builtin_bit_cast(bf16x8, b), c, 0, 0, 0); }
__device__ __forceinline__ f32x4 mfma16(v2u a, v2u b, f32x4 c) { return __builtin_amdgcn_mfma_f32_16x16x16bf16_1k(__builtin_bit_cast(s16x4, a), __builtin_bit_cast(s16x4, b), c, 0, 0, 0); }
__device__ __forceinline__ float silu_f(float v) { return v * __builtin_amdgcn_rcpf(1.f + __builtin_amdgcn_exp2f(-v * LOG2E)); }
__device__ __forceinline__ rsrc_t mk(const void* p, int bytes) { return __builtin_amdgcn_make_buffer_rsrc((void*)p, 0, bytes, 0x00020000); }
__device__ __forceinline__ void dma_nt(rsrc_t rs, unsigned voff, unsigned soff, unsigned ldsaddr) {
  asm volatile("s_mov_b32 m0, %0\n\ts_nop 3\n\tbuffer_load_dwordx4 %1, %2, %3 offen nt lds" ::"s"(ldsaddr), "v"(voff), "s"(rs), "s"(soff) : "memory");
}
__device__ __forceinline__ void dma_df(rsrc_t rs, unsigned voff, unsigned soff, unsigned ldsaddr) {
  asm volatile("s_mov_b32 m0, %0\n\ts_nop 3\n\tbuffer_load_dwordx4 %1, %2, %3 offen lds" ::"s"(ldsaddr), "v"(voff), "s"(rs), "s"(soff) : "memory");
}
#define WPOL 2
#define DMA_W dma_nt
#define KVPOL 2
#define DMA_K dma_nt
#define VMP() asm volatile("s_waitcnt vmcnt(%0)" ::"n"(DEPTH - 1) : "memory")
#define VM0() asm volatile("s_waitcnt vmcnt(0)" ::: "memory")
#define LG0() asm volatile("s_waitcnt lgkmcnt(0)" ::: "memory")
#define CBAR() asm volatile("" ::: "memory")
__device__ __forceinline__ void wg_barrier() { LG0(); __builtin_amdgcn_s_barrier(); CBAR(); }
__device__ __forceinline__ v4u ldh(rsrc_t rs, int off) { return __builtin_amdgcn_raw_buffer_load_b128(rs, off, 0, SC1); }
__device__ __forceinline__ void sth(rsrc_t rs, int off, v4u v) { __builtin_amdgcn_raw_buffer_store_b128(v, rs, off, 0, SC1); }
__device__ __forceinline__ unsigned lflag(lptr L, int f) { return __hip_atomic_load((LAS unsigned*)(L + L_FLAGS) + f, __ATOMIC_RELAXED, __HIP_MEMORY_SCOPE_WORKGROUP); }
__device__ __forceinline__ void lwait(lptr L, int f, unsigned want) {
#pragma unroll 1
  for (unsigned i = 0; i < LSPIN; ++i) { if ((unsigned)__builtin_amdgcn_readfirstlane(lflag(L, f)) >= want) break; __builtin_amdgcn_s_sleep(1); }
  CBAR();
}
__device__ __forceinline__ unsigned larrive(int f) {
  unsigned old;
  asm volatile("s_mov_b64 exec, 1\n\tds_add_rtn_u32 %0, %1, %2\n\ts_mov_b64 exec, -1\n\ts_waitcnt lgkmcnt(0)" : "=&v"(old) : "v"(L_FLAGS + 4 * f), "v"(1u) : "memory");
  return __builtin_amdgcn_readfirstlane(old);
}
__device__ __forceinline__ void lsignal(int f) {
  asm volatile("s_mov_b64 exec, 1\n\tds_add_u32 %0, %1\n\ts_mov_b64 exec, -1" ::"v"(L_FLAGS + 4 * f), "v"(1u) : "memory");
}
__device__ __forceinline__ v4u trfrag(lptr p, int rs4) {
  s16x4 a = __builtin_amdgcn_ds_read_tr16_b64_v4i16((LAS s16x4*)p), b = __builtin_amdgcn_ds_read_tr16_b64_v4i16((LAS s16x4*)(p + rs4));
  v2u x = __builtin_bit_cast(v2u, a), y = __builtin_bit_cast(v2u, b);
  return v4u{x.x, x.y, y.x, y.y};
}
template <int BITS> __device__ __forceinline__ v4u tagv(unsigned tag) { constexpr unsigned m = (1u << BITS) - 1u; return v4u{tag & m, (tag + 1u) & m, (tag + 2u) & m, (tag + 3u) & m}; }
template <int BITS> __device__ __forceinline__ v4u enc(f32x4 v, unsigned tag) {
  constexpr unsigned m = (1u << BITS) - 1u;
  return ((__builtin_bit_cast(v4u, v) + (1u << (BITS - 1))) & ~m) | tagv<BITS>(tag);
}
template <int BITS, int N, int SLEEP> __device__ __forceinline__ void gather(rsrc_t rs, const int (&off)[N], v4u (&v)[N], unsigned tag) {
  const v4u tv = tagv<BITS>(tag);
#pragma unroll 1
  for (unsigned i = 0; i < GSPIN; ++i) {
    CBAR();
#pragma unroll
    for (int k = 0; k < N; ++k) v[k] = ldh(rs, off[k]);
    unsigned bad = 0;
#pragma unroll
    for (int k = 0; k < N; ++k) { v[k] ^= tv; bad |= v[k].x | v[k].y | v[k].z | v[k].w; }
    if (__all((bad & ((1u << BITS) - 1u)) == 0u)) break;
    if (SLEEP) __builtin_amdgcn_s_sleep(SLEEP);
  }
}
__device__ __forceinline__ f32x4 asf(v4u v) { return __builtin_bit_cast(f32x4, v); }
__device__ __forceinline__ void make_sel(unsigned (&sel)[2][4], int lane) {
  const int jp = lane & 7;
#pragma unroll
  for (int hs = 0; hs < 2; ++hs)
#pragma unroll
    for (int d = 0; d < 4; ++d) {
      const unsigned b01 = hs ? 0x0302u : 0x0100u;
      sel[hs][d] = (d == (jp >> 1)) ? ((jp & 1) ? ((b01 << 16) | 0x0c0cu) : (0x0c0c0000u | b01)) : 0x0c0c0c0cu;
    }
}
__device__ __forceinline__ v4u diag(unsigned src, const unsigned (&s)[4]) {
  return v4u{__builtin_amdgcn_perm(src, src, s[0]), __builtin_amdgcn_perm(src, src, s[1]), __builtin_amdgcn_perm(src, src, s[2]), __builtin_amdgcn_perm(src, src, s[3])};
}
__device__ __forceinline__ void gemv_unit(lptr L, rsrc_t rsW, const v4u (&wf)[4], v4u af, int cw, int lane, int flag, int rd, int rd2, int st, int st2, unsigned tag) {
  const int G = lane >> 4, i16 = lane & 15;
  f32x4 acc[4];
#pragma unroll
  for (int t = 0; t < 4; ++t) acc[t] = mfma(wf[t], af, f32x4{0.f, 0.f, 0.f, 0.f});
  if (i16 < 8) {
#pragma unroll
    for (int t = 0; t < 4; ++t) *(LAS f32x4*)(L + L_RED + ((cw * 8 + i16) * 64 + 16 * t + 4 * G) * 4) = acc[t];
  }
  if (larrive(flag) == 3u) {
    f32x4 p[2][4];
#pragma unroll
    for (int q = 0; q < 2; ++q)
#pragma unroll
      for (int k = 0; k < 4; ++k) p[q][k] = *(LAS f32x4*)(L + L_RED + k * 2048 + (rd + q * rd2) * 16);
#pragma unroll
    for (int q = 0; q < 2; ++q) sth(rsW, st + q * st2, enc<4>((p[q][0] + p[q][1]) + (p[q][2] + p[q][3]), tag));
  }
}

template <int NS> __device__ __forceinline__ void c_load_w(lptr L, v4u (&wf)[NS][8], int k0, int lane) {
#pragma unroll
  for (int q = 0; q < NS; ++q)
#pragma unroll
    for (int i = 0; i < 8; ++i) wf[q][i] = *(LAS v4u*)(L + ((lane >> 3) & 1) * 65536 + (k0 + 32 * q + 8 * (lane >> 4) + i) * 128 + (lane & 7) * 16);
}
template <int NS> __device__ __forceinline__ void c_gate_up(lptr L, const v4u (&wf)[NS][8], const unsigned (&sel)[2][4], int k0, int slot, int lane) {
  v4u hs[NS];
#pragma unroll
  for (int q = 0; q < NS; ++q) hs[q] = *(LAS v4u*)(L + L_RED + ((lane >> 3) & 1) * 1024 + (k0 + 32 * q + 8 * (lane >> 4)) * 2);
  f32x4 acc[2] = {{0.f, 0.f, 0.f, 0.f}, {0.f, 0.f, 0.f, 0.f}};
#pragma unroll
  for (int q = 0; q < NS; ++q)
#pragma unroll
    for (int i = 0; i < 8; ++i) acc[i & 1] = mfma(diag(hs[q][i >> 1], sel[i & 1]), wf[q][i], acc[i & 1]);
  *(LAS f32x4*)(L + L_OB + slot * 1024 + lane * 16) = acc[0] + acc[1];
}

struct Args {
  const unsigned short *x, *kc, *vc, *wq, *wo, *wg, *wu, *wd, *c, *wm;
  unsigned short* out;
  unsigned char* ws;
};

#define STAMP(i) do {} while (0)
#define EXIT() return
#define DEPTHV 10
#define DEPTHW 5
#define T_H1 380
#define T_H2 150
template <int N> __device__ __forceinline__ void vmw() { asm volatile("s_waitcnt vmcnt(%0)" ::"n"(N) : "memory"); }
__device__ __forceinline__ unsigned now() { return (unsigned)__builtin_amdgcn_s_memrealtime(); }
__device__ __forceinline__ void pause_until(unsigned t) {
#pragma unroll 1
  for (int i = 0; i < 4096 && (int)(now() - t) < 0; ++i) __builtin_amdgcn_s_sleep(2);
}

}

extern "C" __global__ void __launch_bounds__(1024, 4) decode_block(Args a) {
  extern __shared__ __attribute__((aligned(16))) unsigned char smem[];
  const lptr L = (lptr)smem;
  const int lane = threadIdx.x & 63, w = __builtin_amdgcn_readfirstlane(threadIdx.x >> 6);
  const int wg = blockIdx.x, xc = wg & 7, r = wg >> 3;
  const int G = lane >> 4, i16 = lane & 15;
  const size_t kvoff = ((size_t)((r >> 2) * 8 + xc) * SK + (SK - WIN) + 512 * (r & 3)) * 128;
  const rsrc_t rsW = mk(a.ws, WS_TOTAL);
  const bool isC = r >= 16 && r < 30;
  const int cidx = 14 * xc + (r - 16), fg = cidx >> 2, cb0 = 2 * (cidx & 3);
  STAMP(0);
  unsigned tag;

  if (w >= 8) {
    const int v = w - 8, j = v & 3, hf = v >> 2;
    const unsigned tv0 = now();
    if (v == 0 && lane < F_N) ((LAS unsigned*)(L + L_FLAGS))[lane] = 0u;
    if (v == 7) {
      u64 pc; unsigned rel;
      asm volatile("s_getpc_b64 %0\nLpf_here:\n\ts_mov_b32 %1, Lpf_here-decode_block" : "=s"(pc), "=s"(rel));
      dma_df(mk((const void*)(pc - rel), CODEPF * 1024), lane * 16, r * 1024, L_OB + 7 * 2048);
    }
    wg_barrier();
    const rsrc_t rsV = mk(a.vc + kvoff, 131072);
    v4u vr[16];
    if (hf == 1) {
      const v4u z = {0u, 0u, 0u, 0u};
      if (j < 2) sth(rsW, WS_OP + (xc * 32 + r) * 2048 + (j * 64 + lane) * 16, z);
      if (r < 16) { if (j == 2) sth(rsW, WS_AP + (((4 * (r >> 3) + G) * 8 + xc) * 512 + 64 * (r & 7) + 4 * i16) * 4, z); }
      else if (isC) sth(rsW, WS_FP + (((cb0 + (G >> 1)) * 16 + 4 * j + (i16 >> 2)) * 28 + fg) * 128 + ((G & 1) * 4 + (i16 & 3)) * 16, z);
    }
    if (PFC && hf == 1) {
      if (T_PF) pause_until(tv0 + T_PF);
      const int fg0 = (14 * xc) >> 2;
      if (PFC & 1) {
        const rsrc_t rsG = mk(a.wg, 512 * FF * 2), rsU = mk(a.wu, 512 * FF * 2);
#pragma unroll
        for (int i = 0; i < 2; ++i) {
          dma_df(rsG, (lane >> 5) * (FF * 2) + (lane & 31) * 16, (16 * r + 4 * j + 2 * i) * (FF * 2) + 128 * fg0, L_OB + v * 2048);
          dma_df(rsU, (lane >> 5) * (FF * 2) + (lane & 31) * 16, (16 * r + 4 * j + 2 * i) * (FF * 2) + 128 * fg0, L_OB + v * 2048 + 1024);
        }
      }
      if (PFC & 2) {
        const rsrc_t rsD = mk(a.wd, FF * 512 * 2);
#pragma unroll
        for (int i = 0; i < 2; ++i) dma_df(rsD, lane * 16, (64 * fg0 + 8 * r + 2 * j + i) * 1024, L_OB + v * 2048 + i * 1024);
      }
      if (PFC & 4) {
        const rsrc_t rsO = mk(a.wo, 4096 * 512 * 2);
#pragma unroll
        for (int i = 0; i < 4; ++i) { dma_df(rsO, lane * 16, (512 * xc + 16 * r + 4 * j + i) * 1024, L_OB + v * 2048 + (i & 1) * 1024); }
      }
    }
    lwait(L, F_KI + j, 1 + hf);
#pragma unroll
    for (int i = 0; i < 16; ++i) { vr[i] = __builtin_amdgcn_raw_buffer_load_b128(rsV, lane * 16 + j * 1024, (hf * 16 + i) * 4096, KVPOL); if (hf) vmw<DEPTHV - 1>(); else vmw<DEPTH - 1>(); }
    __builtin_amdgcn_sched_barrier(0);
    lsignal(F_KI + j);
    lsignal(F_VI);
    __builtin_amdgcn_s_setprio(3);
    STAMP(1);
    unsigned s2[2][2];
#pragma unroll
    for (int hs = 0; hs < 2; ++hs)
#pragma unroll
      for (int d = 0; d < 2; ++d) {
        const unsigned b01 = hs ? 0x0302u : 0x0100u;
        s2[hs][d] = (d == ((lane >> 1) & 1)) ? ((lane & 1) ? ((b01 << 16) | 0x0c0cu) : (0x0c0c0000u | b01)) : 0x0c0c0c0cu;
      }
    lwait(L, F_L, 4);
    STAMP(2);
    const lptr pb = L + L_PB + (((j * 2 + hf) * 4 + G) * 4 + (i16 >> 2)) * 32;
    const v4u pw[2] = {*(LAS v4u*)pb, *(LAS v4u*)(pb + 16)};
    float fs;
    {
      const LAS float* ms = (const LAS float*)(L + L_MST) + G * 2;
      const float M = fmaxf(fmaxf(ms[0], ms[8]), fmaxf(ms[16], ms[24]));
      fs = __builtin_amdgcn_exp2f((ms[j * 8] - M) * LOG2E);
    }
    tag = __builtin_amdgcn_readfirstlane(lflag(L, F_TAG));
    f32x4 acc[2] = {{0.f, 0.f, 0.f, 0.f}, {0.f, 0.f, 0.f, 0.f}};
#pragma unroll
    for (int i = 0; i < 16; ++i) {
      const unsigned src = pw[i >> 3][(i >> 1) & 3];
      const v2u A = {__builtin_amdgcn_perm(src, src, s2[i & 1][0]), __builtin_amdgcn_perm(src, src, s2[i & 1][1])};
      acc[0] = mfma16(A, v2u{vr[i].x, vr[i].y}, acc[0]);
      acc[1] = mfma16(A, v2u{vr[i].z, vr[i].w}, acc[1]);
    }
    STAMP(3);
#pragma unroll
    for (int q = 0; q < 2; ++q) *(LAS f32x4*)(L + L_OB + v * 2048 + lane * 32 + q * 16) = acc[q] * fs;
    if (larrive(F_V) == 7u) {
      f32x4 p[2][8];
#pragma unroll
      for (int q = 0; q < 2; ++q)
#pragma unroll
        for (int k = 0; k < 8; ++k) p[q][k] = *(LAS f32x4*)(L + L_OB + k * 2048 + (q * 64 + lane) * 16);
#pragma unroll
      for (int q = 0; q < 2; ++q)
        sth(rsW, WS_OP + (xc * 32 + r) * 2048 + (q * 64 + lane) * 16, enc<8>(((p[q][0] + p[q][1]) + (p[q][2] + p[q][3])) + ((p[q][4] + p[q][5]) + (p[q][6] + p[q][7])), tag));
      lsignal(F_PUB);
      STAMP(4);
    }
    if (!isC || ABL >= 1) EXIT();
    unsigned sel[2][4];
    make_sel(sel, lane);
    if (hf != 0) {
      wg_barrier();
      v4u wf[2][8];
      c_load_w<2>(L, wf, 256 + 64 * j, lane);
      wg_barrier();
      wg_barrier();
      c_gate_up<2>(L, wf, sel, 256 + 64 * j, 8 + j, lane);
      wg_barrier();
      EXIT();
    }
    lwait(L, F_PUB, 1);
    v4u dw[16];
    {
      const rsrc_t rsD = mk(a.wd, FF * 512 * 2);
#pragma unroll
      for (int i = 0; i < 16; ++i) dw[i] = __builtin_amdgcn_raw_buffer_load_b128(rsD, G * 16384 + i16 * 16 + j * 256, (64 * fg + i) * 1024, WPOL);
      __builtin_amdgcn_sched_barrier(0);
    }
    wg_barrier();
    wg_barrier();
    wg_barrier();
    wg_barrier();
    f32x4 p[12];
#pragma unroll
    for (int k = 0; k < 12; ++k) p[k] = *(LAS f32x4*)(L + L_OB + k * 1024 + lane * 16);
    const f32x4 gu = (((p[0] + p[1]) + (p[2] + p[3])) + ((p[4] + p[5]) + (p[6] + p[7]))) + ((p[8] + p[9]) + (p[10] + p[11]));
    float av[4];
#pragma unroll
    for (int d = 0; d < 4; ++d) { const float t = rb(gu[d]); av[d] = rb(silu_f(t)) * dpp<ROR8>(t); }
    const lptr ab = L + L_RED + 2048 + j * 512;
    *(LAS v2u*)(ab + (G >> 1) * 256 + (8 * i16 + 4 * (G & 1)) * 2) = v2u{pk2(av[0], av[1]), pk2(av[2], av[3])};
    CBAR();
    const v4u a0 = *(LAS v4u*)(ab + ((lane >> 3) & 1) * 256 + G * 32), a1 = *(LAS v4u*)(ab + ((lane >> 3) & 1) * 256 + G * 32 + 16);
    f32x4 fa[2] = {{0.f, 0.f, 0.f, 0.f}, {0.f, 0.f, 0.f, 0.f}};
#pragma unroll
    for (int i = 0; i < 16; ++i) fa[i & 1] = mfma(diag(i < 8 ? a0[(i >> 1) & 3] : a1[(i >> 1) & 3], sel[i & 1]), dw[i], fa[i & 1]);
    sth(rsW, WS_FP + (((cb0 + (G >> 1)) * 16 + 4 * j + (i16 >> 2)) * 28 + fg) * 128 + ((G & 1) * 4 + (i16 & 3)) * 16, enc<8>(fa[0] + fa[1], tag));
    STAMP(10);
    EXIT();
  } else if (w >= 4) {
    const int j = w - 4;
    const rsrc_t rsK = mk(a.kc + kvoff, 131072);
    unsigned voff[4];
#pragma unroll
    for (int q = 0; q < 4; ++q) voff[q] = j * 1024 + G * 256 + ((i16 ^ (4 * q + G)) << 4);
#pragma unroll
    for (int i = 0; i < DEPTH - 1; ++i) DMA_K(rsK, voff[i & 3], i * 4096, i * 4096 + j * 1024);
    wg_barrier();
#pragma unroll
    for (int i = DEPTH - 1; i < 24; ++i) { DMA_K(rsK, voff[i & 3], i * 4096, i * 4096 + j * 1024); VMP(); }
    lwait(L, F_EW, 4);
#pragma unroll
    for (int i = 24; i < 32; ++i) { DMA_K(rsK, voff[i & 3], i * 4096, i * 4096 + j * 1024); VMP(); }
    lsignal(F_KI + j);
    STAMP(1);
    VM0();
    STAMP(2);
    const lptr kb = L + j * 1024 + (i16 >> 2) * 4096 + (i16 & 3) * 256;
    v4u kf[8][4];
#define KREAD(u) _Pragma("unroll") for (int ks = 0; ks < 4; ++ks) kf[u][ks] = *(LAS v4u*)(kb + (u) * 16384 + (((4 * ks + G) ^ i16) << 4))
    KREAD(0); KREAD(4); KREAD(1); KREAD(5); KREAD(2);
    __builtin_amdgcn_sched_barrier(0);
    lwait(L, F_QR, 4);
    STAMP(3);
    const int h = i16 & 3, rho = i16 >> 2;
    v4u qf[4];
#pragma unroll
    for (int ks = 0; ks < 4; ++ks) qf[ks] = *(LAS v4u*)(L + L_QS + h * 256 + ks * 64 + G * 16);
    tag = lflag(L, F_TAG);
    f32x4 sc[2] = {{0.f, 0.f, 0.f, 0.f}, {0.f, 0.f, 0.f, 0.f}};
#define KMMA(rp) _Pragma("unroll") for (int ks = 0; ks < 4; ++ks) { const v4u qm = (rho == (rp)) ? qf[ks] : v4u{0u, 0u, 0u, 0u}; \
      sc[0] = mfma(kf[rp][ks], qm, sc[0]); sc[1] = mfma(kf[4 + (rp)][ks], qm, sc[1]); }
    __builtin_amdgcn_sched_barrier(0);
    KMMA(0);
    __builtin_amdgcn_sched_barrier(0);
    KREAD(6); KREAD(3);
    __builtin_amdgcn_sched_barrier(0);
    KMMA(1);
    __builtin_amdgcn_sched_barrier(0);
    KREAD(7);
    __builtin_amdgcn_sched_barrier(0);
    KMMA(2); KMMA(3);
    tag = __builtin_amdgcn_readfirstlane(tag);
    float s[8];
#pragma unroll
    for (int k = 0; k < 8; ++k) s[k] = rb(rb(sc[k >> 2][k & 3]) * 0.08838834764831845f);
    float m = fmaxf(fmaxf(fmaxf(s[0], s[1]), fmaxf(s[2], s[3])), fmaxf(fmaxf(s[4], s[5]), fmaxf(s[6], s[7])));
    m = fmaxf(m, dpp<ROR4>(m)); m = fmaxf(m, dpp<ROR8>(m)); m = x32_max(x16_max(m));
    float l = 0.f;
#pragma unroll
    for (int k = 0; k < 8; ++k) {
      const float p = __builtin_amdgcn_exp2f((s[k] - m) * LOG2E);
      l += p;
      *(LAS __bf16*)(L + L_PB + ((((j * 2 + (k >> 2)) * 4 + (k & 3)) * 4 + h) * 16 + 4 * rho + G) * 2) = (__bf16)p;
    }
    l += dpp<ROR4>(l); l += dpp<ROR8>(l); l = x32_sum(x16_sum(l));
    if (lane < 4) { ((LAS float*)(L + L_MST))[(j * 4 + lane) * 2] = m; ((LAS float*)(L + L_MST))[(j * 4 + lane) * 2 + 1] = l; }
    if (larrive(F_L) == 3u) {
      if (lane < 4) {
        const LAS float* ms = (const LAS float*)(L + L_MST) + lane * 2;
        const float M = fmaxf(fmaxf(ms[0], ms[8]), fmaxf(ms[16], ms[24]));
        float Ls = 0.f;
#pragma unroll
        for (int k = 0; k < 4; ++k) Ls += ms[k * 8 + 1] * __builtin_amdgcn_exp2f((ms[k * 8] - M) * LOG2E);
        sth(rsW, WS_ST + (((xc * 8 + (r >> 2)) * 4 + lane) * 4 + (r & 3)) * 16, v4u{__float_as_uint(M), tag, __float_as_uint(Ls), ~tag});
      }
    }
    STAMP(4);
  } else {
    const int cw = w;
    const unsigned tstart = now();
    const bool isM = r < 24;
    const int ksq = r & 3, cgq = r >> 2;
    const int mg = cgq < 2 ? 2 * xc + cgq : 16 + 4 * xc + (cgq - 2);
    const rsrc_t rsM = mk(a.wm, 512 * 3072 * 2), rsQ = mk(a.wq, 512 * 4096 * 2), rsC = mk(a.c, 8192), rsX = mk(a.x, 8192);
    const int krow = 128 * ksq + 32 * cw;
    const unsigned ep = __builtin_amdgcn_raw_buffer_load_b32(rsW, WS_EPOCH, 0, SC1);
    const v4u cf = __builtin_amdgcn_raw_buffer_load_b128(rsC, (lane & 7) * 1024 + (krow + 8 * G) * 2, 0, 0);
    v4u xr[2];
#pragma unroll
    for (int i = 0; i < 2; ++i) xr[i] = __builtin_amdgcn_raw_buffer_load_b128(rsX, (2 * cw + i) * 1024 + lane * 16, 0, 0);
    const v4u xf = __builtin_amdgcn_raw_buffer_load_b128(rsX, (lane & 7) * 1024 + (krow + 8 * G) * 2, 0, 0);
    __builtin_amdgcn_sched_barrier(0);
    const bool crit = cgq < 2;
#define DMA_M() _Pragma("unroll") for (int i = 0; i < 4; ++i) dma_nt(rsM, (lane >> 3) * 6144 + (lane & 7) * 16, (krow + 8 * i) * 6144 + 128 * mg, L_EM + cw * 4096 + i * 1024)
#define DMA_Q() _Pragma("unroll") for (int i = 0; i < 4; ++i) dma_nt(rsQ, (lane >> 3) * 8192 + (lane & 7) * 16, (krow + 8 * i) * 8192 + (512 * xc + 64 * cgq) * 2, L_EQ + cw * 4096 + i * 1024)
    if (isM) DMA_M();
    DMA_Q();
    wg_barrier();
    tag = __builtin_amdgcn_readfirstlane(ep) + 1u;
    v4u sf;
#pragma unroll
    for (int d = 0; d < 4; ++d) sf[d] = pk2(silu_f(bflo(cf[d])), silu_f(bfhi(cf[d])));
    asm volatile("" : "+v"(sf));
    asm volatile("s_waitcnt vmcnt(4)" ::: "memory");
    STAMP(1);
    if (cw == 0 && lane == 0) ((LAS unsigned*)(L + L_FLAGS))[F_TAG] = tag;
    const int troff = cw * 4096 + (8 * G + (i16 >> 2)) * 128 + (i16 & 3) * 8;
    if (isM) {
      v4u wmf[4];
#pragma unroll
      for (int t = 0; t < 4; ++t) wmf[t] = trfrag(L + L_EM + troff + t * 32, 4 * 128);
      const int rd = crit ? (lane & 7) * 16 + (lane >> 3) : lane, rd2 = crit ? 8 : 64;
      const int st = crit ? WS_MC + (ksq * 16 + 2 * (mg & 7)) * 2048 + ((lane >> 3) & 1) * 1024 + (lane >> 4) * 256 + (mg >> 3) * 128 + (lane & 7) * 16
                          : WS_MP + ((ksq * 8 + G) * 2048 + 64 * (mg - 16) + 4 * i16) * 4;
      gemv_unit(L, rsW, wmf, sf, cw, lane, F_M, rd, rd2, st, crit ? 2048 : 4 * 2048 * 4, tag);
    }
    STAMP(2);
    VM0();
    v4u wqf[4];
#pragma unroll
    for (int t = 0; t < 4; ++t) wqf[t] = trfrag(L + L_EQ + troff + t * 32, 4 * 128);
    LG0();
    asm volatile("" : "+v"(wqf[0]), "+v"(wqf[1]), "+v"(wqf[2]), "+v"(wqf[3]));
    lsignal(F_EW);
#pragma unroll
    for (int i = 0; i < 2; ++i) {
      float e[8];
#pragma unroll
      for (int d = 0; d < 4; ++d) { e[2 * d] = bflo(xr[i][d]); e[2 * d + 1] = bfhi(xr[i][d]); }
      const float mean = wave_sum(((e[0] + e[1]) + (e[2] + e[3])) + ((e[4] + e[5]) + (e[6] + e[7]))) * (1.f / 512.f);
      float q = 0.f;
#pragma unroll
      for (int d = 0; d < 8; ++d) q += (e[d] - mean) * (e[d] - mean);
      const float var = wave_sum(q) * (1.f / 512.f);
      if (lane == 0) { ((LAS float*)(L + L_STAT))[(2 * cw + i) * 2] = rb(mean); ((LAS float*)(L + L_STAT))[(2 * cw + i) * 2 + 1] = rb(__builtin_amdgcn_sqrtf(rb(rb(var) + 1e-5f))); }
    }
    lsignal(F_LN);
    lwait(L, F_LN, 4);
    float nx[8];
    {
      const float mu = ((LAS float*)(L + L_STAT))[(lane & 7) * 2], rd = __builtin_amdgcn_rcpf(((LAS float*)(L + L_STAT))[(lane & 7) * 2 + 1]);
#pragma unroll
      for (int d = 0; d < 4; ++d) { nx[2 * d] = rb(rb(bflo(xf[d]) - mu) * rd); nx[2 * d + 1] = rb(rb(bfhi(xf[d]) - mu) * rd); }
    }
    {
      v4u g[8];
      const int o0 = WS_MC + (4 * ksq + cw) * 2048 + lane * 16;
      constexpr int PS = 16 * 2048;
      const int off[8] = {o0, o0 + 1024, o0 + PS, o0 + PS + 1024, o0 + 2 * PS, o0 + 2 * PS + 1024, o0 + 3 * PS, o0 + 3 * PS + 1024};
      pause_until(tstart + T_H1);
      if (ABL < 2) gather<4, 8, 16>(rsW, off, g, tag);
      STAMP(3);
      const f32x4 m0 = (asf(g[0]) + asf(g[2])) + (asf(g[4]) + asf(g[6])), m1 = (asf(g[1]) + asf(g[3])) + (asf(g[5]) + asf(g[7]));
      float hv[8];
#pragma unroll
      for (int d = 0; d < 8; ++d) {
        const float sa = rb(d < 4 ? m0[d & 3] : m1[d & 3]), ca = dpp<ROR8>(sa);
        hv[d] = rb(nx[d] * rb(1.f + ca)) + sa;
      }
      const v4u hf = {pk2(hv[0], hv[1]), pk2(hv[2], hv[3]), pk2(hv[4], hv[5]), pk2(hv[6], hv[7])};
      gemv_unit(L, rsW, wqf, hf, cw, lane, F_Q, lane, 64, WS_QP + ((ksq * 8 + G) * 4096 + 512 * xc + 64 * cgq + 4 * i16) * 4, 4 * 4096 * 4, tag);
    }
    STAMP(4);
    {
      v4u g[2];
      const int o0 = WS_QP + (((lane >> 5) * 8 + (r >> 2)) * 4096 + 512 * xc + 128 * cw + 4 * (lane & 31)) * 4;
      const int off[2] = {o0, o0 + 2 * 8 * 4096 * 4};
      pause_until(now() + T_H2);
      if (ABL < 2) gather<4, 2, 16>(rsW, off, g, tag);
      const f32x4 s = asf(g[0]) + asf(g[1]);
      const float q0 = x32_sum(s.x), q1 = x32_sum(s.y), q2 = x32_sum(s.z), q3 = x32_sum(s.w);
      if (lane < 32) *(LAS v2u*)(L + L_QS + cw * 256 + lane * 8) = v2u{pk2(q0, q1), pk2(q2, q3)};
      lsignal(F_QR);
    }
    STAMP(5);
    if (cw == 0 && ABL < 2) {
      v4u g[1];
      const int off[1] = {WS_MP + ((G * 8 + xc) * 2048 + 64 * r + 4 * i16) * 4};
      gather<4, 1, 8>(rsW, off, g, tag);
      const f32x4 s = asf(g[0]);
      f32x4 o;
#pragma unroll
      for (int d = 0; d < 4; ++d) o[d] = rb(x32_sum(x16_sum(s[d])));
      if (lane < 16) sth(rsW, WS_MOD + (xc * 2048 + 64 * r + 4 * i16) * 4, __builtin_bit_cast(v4u, o) | tagv<16>(tag));
    }
  }

  if (r >= 30 || ABL >= 1) EXIT();
  const int tw = w;
  lwait(L, F_L, 4);
  lwait(L, F_VI, 8);
  STAMP(6);
  if (r < 16) {
    const int cg = r & 7, bg = r >> 3;
    {
      const rsrc_t rsO = mk(a.wo, 4096 * 512 * 2);
#pragma unroll
      for (int i = 0; i < 8; ++i) DMA_W(rsO, (lane >> 3) * 1024 + (lane & 7) * 16, (512 * xc + 64 * tw + 8 * i) * 1024 + 128 * cg, tw * 8192 + i * 1024), vmw<DEPTHW - 1>();
    }
    STAMP(14);
    float scl[4];
    {
      v4u g;
#pragma unroll 1
      for (unsigned i = 0; i < GSPIN; ++i) {
        CBAR();
        g = ldh(rsW, WS_ST + (((xc * 8 + 4 * bg + G) * 4 + (tw >> 1)) * 4 + (lane & 3)) * 16);
        if (__all(g.y == tag && g.w == ~tag)) break;
        __builtin_amdgcn_s_sleep(2);
      }
      const float ms = __uint_as_float(g.x), ls = __uint_as_float(g.z);
      float M = fmaxf(ms, dpp<XOR1>(ms)); M = fmaxf(M, dpp<XOR2>(M));
      const float e = __builtin_amdgcn_exp2f((ms - M) * LOG2E);
      float den = e * ls; den += dpp<XOR1>(den); den += dpp<XOR2>(den);
      const float sc = e * __builtin_amdgcn_rcpf(den);
      scl[0] = dpp<0x00>(sc); scl[1] = dpp<0x55>(sc); scl[2] = dpp<0xAA>(sc); scl[3] = dpp<0xFF>(sc);
    }
    STAMP(15);
    VM0();
    v4u wf[2][4];
#pragma unroll
    for (int kk = 0; kk < 2; ++kk)
#pragma unroll
      for (int t = 0; t < 4; ++t) wf[kk][t] = trfrag(L + tw * 8192 + (32 * kk + 8 * G + (i16 >> 2)) * 128 + t * 32 + (i16 & 3) * 8, 4 * 128);
    STAMP(7);
    lwait(L, F_PUB, 1);
    STAMP(8);
    {
      v4u g[4];
      const int o0 = WS_OP + (xc * 32 + (4 * bg + G) * 4) * 2048 + tw * 256 + i16 * 16;
      const int off[4] = {o0, o0 + 2048, o0 + 4096, o0 + 6144};
      gather<8, 4, 0>(rsW, off, g, tag);
      STAMP(9);
      const f32x4 o = (asf(g[0]) * scl[0] + asf(g[1]) * scl[1]) + (asf(g[2]) * scl[2] + asf(g[3]) * scl[3]);
      const lptr ob = L + 65536 + tw * 512;
      *(LAS v2u*)(ob + lane * 8) = v2u{pk2(o.x, o.y), pk2(o.z, o.w)};
      CBAR();
      const v4u of0 = *(LAS v4u*)(ob + (lane & 3) * 128 + G * 16), of1 = *(LAS v4u*)(ob + (lane & 3) * 128 + 64 + G * 16);
      f32x4 acc[4];
#pragma unroll
      for (int t = 0; t < 4; ++t) acc[t] = mfma(wf[0][t], of0, f32x4{0.f, 0.f, 0.f, 0.f});
#pragma unroll
      for (int t = 0; t < 4; ++t) acc[t] = mfma(wf[1][t], of1, acc[t]);
      const int rep = i16 >> 2;
      const f32x4 d = rep == 0 ? acc[0] : rep == 1 ? acc[1] : rep == 2 ? acc[2] : acc[3];
      *(LAS f32x4*)(L + L_RED + tw * 1024 + (lane & 3) * 256 + rep * 64 + G * 16) = d;
    }
    wg_barrier();
    if (tw == 0) {
      f32x4 p[8];
#pragma unroll
      for (int k = 0; k < 8; ++k) p[k] = *(LAS f32x4*)(L + L_RED + k * 1024 + lane * 16);
      sth(rsW, WS_AP + (((4 * bg + G) * 8 + xc) * 512 + 64 * cg + 4 * i16) * 4, enc<8>(((p[0] + p[1]) + (p[2] + p[3])) + ((p[4] + p[5]) + (p[6] + p[7])), tag));
      STAMP(10);
    }
    if (tw != 1) EXIT();
    const int col = 32 * r + 8 * (lane & 3) + (lane & 4), pg = lane >> 3;
    const v2u xx = __builtin_amdgcn_raw_buffer_load_b64(mk(a.x, 8192), (xc * 512 + col) * 2, 0, 0);
    v4u md[2];
    {
      const int off[2] = {WS_MOD + (xc * 2048 + col) * 4, WS_MOD + (xc * 2048 + 1536 + col) * 4};
      gather<16, 2, 2>(rsW, off, md, tag);
    }
    f32x4 x2;
    {
      v4u p[1];
      const int off1[1] = {WS_AP + ((xc * 8 + pg) * 512 + col) * 4};
      gather<8, 1, 2>(rsW, off1, p, tag);
      f32x4 s = asf(p[0]);
#pragma unroll
      for (int d = 0; d < 4; ++d) { float t = s[d]; t += dpp<ROR8>(t); s[d] = x32_sum(x16_sum(t)); }
      const f32x4 ga = asf(md[0]);
      x2.x = rb(bflo(xx.x) + rb(ga.x * rb(s.x))); x2.y = rb(bfhi(xx.x) + rb(ga.y * rb(s.y)));
      x2.z = rb(bflo(xx.y) + rb(ga.z * rb(s.z))); x2.w = rb(bfhi(xx.y) + rb(ga.w * rb(s.w)));
    }
    STAMP(11);
    {
      v4u g[4];
      const int o0 = WS_FP + (xc * 16 + r) * 3584 + lane * 16;
      const int off[4] = {o0, o0 + 1024, o0 + 2048, lane < 32 ? o0 + 3072 : o0};
      gather<8, 4, 0>(rsW, off, g, tag);
      STAMP(12);
      f32x4 s = (asf(g[0]) + asf(g[1])) + asf(g[2]);
      if (lane < 32) s += asf(g[3]);
#pragma unroll
      for (int d = 0; d < 4; ++d) { float t = s[d]; t += dpp<ROR8>(t); s[d] = x32_sum(x16_sum(t)); }
      const f32x4 gb = asf(md[1]);
      const float o0f = x2.x + rb(gb.x * rb(s.x)), o1 = x2.y + rb(gb.y * rb(s.y)), o2 = x2.z + rb(gb.z * rb(s.z)), o3 = x2.w + rb(gb.w * rb(s.w));
      if (lane < 8) __builtin_amdgcn_raw_buffer_store_b64(v2u{pk2(o0f, o1), pk2(o2, o3)}, mk(a.out, 8192), (xc * 512 + col) * 2, 0, 0);
    }
    STAMP(13);
  } else {
    const int b = cb0 + (tw >> 2), col = 128 * (tw & 3) + 4 * (lane & 31);
    {
      const rsrc_t rsG = mk(a.wg, 512 * FF * 2), rsU = mk(a.wu, 512 * FF * 2);
      const int rot = 0;
#pragma unroll
      for (int i = 0; i < 8; ++i) { const int p = (i + rot) & 7;
        DMA_W(rsG, (lane >> 3) * (FF * 2) + (lane & 7) * 16, (64 * tw + 8 * p) * (FF * 2) + 128 * fg, tw * 8192 + p * 1024); vmw<DEPTHW - 1>();
        DMA_W(rsU, (lane >> 3) * (FF * 2) + (lane & 7) * 16, (64 * tw + 8 * p) * (FF * 2) + 128 * fg, 65536 + tw * 8192 + p * 1024); vmw<DEPTHW - 1>(); }
    }
    STAMP(11);
    unsigned sel[2][4];
    make_sel(sel, lane);
    const v2u xx = __builtin_amdgcn_raw_buffer_load_b64(mk(a.x, 8192), (b * 512 + col) * 2, 0, 0);
    v4u md[3];
    {
      const int o0 = WS_MOD + (b * 2048 + col) * 4;
      const int off[3] = {o0, o0 + 512 * 4, o0 + 1024 * 4};
      gather<16, 3, 2>(rsW, off, md, tag);
    }
    VM0();
    STAMP(12);
    wg_barrier();
    v4u wf[1][8];
    c_load_w<1>(L, wf, 32 * tw, lane);
    STAMP(7);
    const f32x4 ga = asf(md[0]), sb = asf(md[1]);
    f32x4 cb1 = asf(md[2]);
#pragma unroll
    for (int d = 0; d < 4; ++d) cb1[d] = rb(1.f + cb1[d]);
    float x2[4];
    {
      v4u g[4];
      const int o0 = WS_AP + ((b * 8 + 4 * (lane >> 5)) * 512 + col) * 4;
      const int off[4] = {o0, o0 + 2048, o0 + 4096, o0 + 6144};
      gather<8, 4, 0>(rsW, off, g, tag);
      STAMP(9);
      const f32x4 s = (asf(g[0]) + asf(g[1])) + (asf(g[2]) + asf(g[3]));
      x2[0] = rb(bflo(xx.x) + rb(ga.x * rb(x32_sum(s.x)))); x2[1] = rb(bfhi(xx.x) + rb(ga.y * rb(x32_sum(s.y))));
      x2[2] = rb(bflo(xx.y) + rb(ga.z * rb(x32_sum(s.z)))); x2[3] = rb(bfhi(xx.y) + rb(ga.w * rb(x32_sum(s.w))));
    }
    if (wg == 128 && tw == 0 && lane == 0) __builtin_amdgcn_raw_buffer_store_b32(tag, rsW, WS_EPOCH, 0, SC1);
    const float c0 = __builtin_bit_cast(float, __builtin_amdgcn_readfirstlane(__builtin_bit_cast(int, x2[0])));
    const float d0 = x2[0] - c0, d1 = x2[1] - c0, d2 = x2[2] - c0, d3 = x2[3] - c0;
    const float s1 = half_sum((d0 + d1) + (d2 + d3)), s2 = half_sum((d0 * d0 + d1 * d1) + (d2 * d2 + d3 * d3));
    if (lane == 0) { ((LAS float*)(L + L_STAT))[tw * 2] = c0 + s1 * (1.f / 128.f); ((LAS float*)(L + L_STAT))[tw * 2 + 1] = s2 - s1 * s1 * (1.f / 128.f); }
    wg_barrier();
    const f32x4 sa = *(LAS f32x4*)(L + L_STAT + (tw >> 2) * 32), sc = *(LAS f32x4*)(L + L_STAT + (tw >> 2) * 32 + 16);
    const float mean = 0.25f * ((sa.x + sa.z) + (sc.x + sc.z));
    const float e0 = sa.x - mean, e1 = sa.z - mean, e2 = sc.x - mean, e3 = sc.z - mean;
    const float var = fmaxf((((sa.y + sa.w) + (sc.y + sc.w)) + 128.f * ((e0 * e0 + e1 * e1) + (e2 * e2 + e3 * e3))) * (1.f / 512.f), 0.f);
    const float mu = rb(mean), rd = __builtin_amdgcn_rcpf(rb(__builtin_amdgcn_sqrtf(rb(rb(var) + 1e-5f))));
    float h2[4];
#pragma unroll
    for (int d = 0; d < 4; ++d) h2[d] = rb(rb(rb(x2[d] - mu) * rd) * cb1[d]) + sb[d];
    if (lane < 32) *(LAS v2u*)(L + L_RED + (tw >> 2) * 1024 + col * 2) = v2u{pk2(h2[0], h2[1]), pk2(h2[2], h2[3])};
    wg_barrier();
    c_gate_up<1>(L, wf, sel, 32 * tw, tw, lane);
    wg_barrier();
    STAMP(8);
  }
  EXIT();
}

extern "C" void kernel_launch(void* const* d_in, const int* in_sizes, int n_in, void* d_out, int out_size, void* d_ws, size_t ws_size, hipStream_t stream) {
  Args a;
  a.x = (const unsigned short*)d_in[0]; a.kc = (const unsigned short*)d_in[1]; a.vc = (const unsigned short*)d_in[2];
  a.wq = (const unsigned short*)d_in[3]; a.wo = (const unsigned short*)d_in[4];
  a.wg = (const unsigned short*)d_in[6]; a.wu = (const unsigned short*)d_in[7]; a.wd = (const unsigned short*)d_in[8];
  a.c = (const unsigned short*)d_in[9]; a.wm = (const unsigned short*)d_in[10];
  a.out = (unsigned short*)d_out; a.ws = (unsigned char*)d_ws;
  (void)hipFuncSetAttribute((const void*)decode_block, hipFuncAttributeMaxDynamicSharedMemorySize, L_TOTAL);
  hipLaunchKernelGGL(decode_block, dim3(256), dim3(1024), L_TOTAL, stream, a);
}
```

```cpp
#define PFC 0
#define DIAG_S 100
#define DIAG_W0 2
#define DIAG_W1 3
#define DIAG_ROLE 0
#include <hip/hip_runtime.h>
#include <hip/hip_bf16.h>
#include <stdint.h>

#define STAMPS 0
#define DEPTH 6
#define T_PF 0
#define ABL 0
#define PREW 1
#define CODEPF 20

namespace {

typedef unsigned v4u __attribute__((ext_vector_type(4)));
typedef unsigned v2u __attribute__((ext_vector_type(2)));
typedef float f32x4 __attribute__((ext_vector_type(4)));
typedef short s16x4 __attribute__((ext_vector_type(4)));
typedef __bf16 bf16x8 __attribute__((ext_vector_type(8)));
typedef __bf16 bf16x2 __attribute__((ext_vector_type(2)));
typedef unsigned long long u64;
typedef __amdgpu_buffer_rsrc_t rsrc_t;
#define LAS __attribute__((address_space(3)))
typedef LAS unsigned char* lptr;

constexpr int SK = 8192, WIN = 2048, FF = 1792;
constexpr int SC1 = 16, NT = 2;
constexpr int WS_EPOCH = 0;
constexpr int WS_MC = 4096;
constexpr int WS_MP = WS_MC + 131072;
constexpr int WS_MOD = WS_MP + 262144;
constexpr int WS_QP = WS_MOD + 65536;
constexpr int WS_OP = WS_QP + 524288;
constexpr int WS_ST = WS_OP + 524288;
constexpr int WS_AP = WS_ST + 16384;
constexpr int WS_FP = WS_AP + 131072;
constexpr int WS_TOTAL = WS_FP + 458752;
constexpr int WS_STAMP = 4 << 20;
constexpr int L_EM = 98304, L_EQ = 114688;
constexpr int L_FLAGS = 131072, L_QS = L_FLAGS + 256, L_MST = L_QS + 1024, L_STAT = L_MST + 128, L_PB = L_STAT + 128, L_OB = L_PB + 4096, L_RED = L_OB + 16384,
              L_TOTAL = L_RED + 8192;
static_assert(L_TOTAL <= 163840, "LDS");
enum { F_EW = 0, F_M = 1, F_LN = 2, F_Q = 3, F_QR = 4, F_L = 5, F_VI = 6, F_V = 7, F_PUB = 8, F_TAG = 9, F_KI = 10, F_N = 16 };
constexpr unsigned GSPIN = 1u << 16, LSPIN = 1u << 20;
constexpr float LOG2E = 1.4426950408889634f;

__device__ __forceinline__ float bflo(unsigned u) { return __uint_as_float(u << 16); }
__device__ __forceinline__ float bfhi(unsigned u) { return __uint_as_float(u & 0xffff0000u); }
__device__ __forceinline__ unsigned pk2(float a, float b) { bf16x2 p = {(__bf16)a, (__bf16)b}; return __builtin_bit_cast(unsigned, p); }
__device__ __forceinline__ float rb(float a) { unsigned u = pk2(a, a); asm("" : "+v"(u)); return bfhi(u); }
template <int CTRL> __device__ __forceinline__ float dpp(float x) { return __builtin_bit_cast(float, __builtin_amdgcn_mov_dpp(__builtin_bit_cast(int, x), CTRL, 0xf, 0xf, true)); }
constexpr int XOR1 = 0xB1, XOR2 = 0x4E, HMIR = 0x141, ROR8 = 0x128, ROR4 = 0x124;
__device__ __forceinline__ float x16_sum(float x) { auto s = __builtin_amdgcn_permlane16_swap(__float_as_uint(x), __float_as_uint(x), false, false); return __uint_as_float(s[0]) + __uint_as_float(s[1]); }
__device__ __forceinline__ float x32_sum(float x) { auto s = __builtin_amdgcn_permlane32_swap(__float_as_uint(x), __float_as_uint(x), false, false); return __uint_as_float(s[0]) + __uint_as_float(s[1]); }
__device__ __forceinline__ float x16_max(float x) { auto s = __builtin_amdgcn_permlane16_swap(__float_as_uint(x), __float_as_uint(x), false, false); return fmaxf(__uint_as_float(s[0]), __uint_as_float(s[1])); }
__device__ __forceinline__ float x32_max(float x) { auto s = __builtin_amdgcn_permlane32_swap(__float_as_uint(x), __float_as_uint(x), false, false); return fmaxf(__uint_as_float(s[0]), __uint_as_float(s[1])); }
__device__ __forceinline__ float half_sum(float x) { x += dpp<XOR1>(x); x += dpp<XOR2>(x); x += dpp<HMIR>(x); x += dpp<ROR8>(x); return x16_sum(x); }
__device__ __forceinline__ float wave_sum(float x) { return x32_sum(half_sum(x)); }
__device__ __forceinline__ f32x4 mfma(v4u a, v4u b, f32x4 c) { return __builtin_amdgcn_mfma_f32_16x16x32_bf16(__builtin_bit_cast(bf16x8, a), __builtin_bit_cast(bf16x8, b), c, 0, 0, 0); }
__device__ __forceinline__ f32x4 mfma16(v2u a, v2u b, f32x4 c) { return __builtin_amdgcn_mfma_f32_16x16x16bf16_1k(__builtin_bit_cast(s16x4, a), __builtin_bit_cast(s16x4, b), c, 0, 0, 0); }
__device__ __forceinline__ float silu_f(float v) { return v * __builtin_amdgcn_rcpf(1.f + __builtin_amdgcn_exp2f(-v * LOG2E)); }
__device__ __forceinline__ rsrc_t mk(const void* p, int bytes) { return __builtin_amdgcn_make_buffer_rsrc((void*)p, 0, bytes, 0x00020000); }
__device__ __forceinline__ void dma_nt(rsrc_t rs, unsigned voff, unsigned soff, unsigned ldsaddr) {
  asm volatile("s_mov_b32 m0, %0\n\ts_nop 3\n\tbuffer_load_dwordx4 %1, %2, %3 offen nt lds" ::"s"(ldsaddr), "v"(voff), "s"(rs), "s"(soff) : "memory");
}
__device__ __forceinline__ void dma_df(rsrc_t rs, unsigned voff, unsigned soff, unsigned ldsaddr) {
  asm volatile("s_mov_b32 m0, %0\n\ts_nop 3\n\tbuffer_load_dwordx4 %1, %2, %3 offen lds" ::"s"(ldsaddr), "v"(voff), "s"(rs), "s"(soff) : "memory");
}
#define WPOL 2
#define DMA_W dma_nt
#define KVPOL 2
#define DMA_K dma_nt
#define VMP() asm volatile("s_waitcnt vmcnt(%0)" ::"n"(DEPTH - 1) : "memory")
#define VM0() asm volatile("s_waitcnt vmcnt(0)" ::: "memory")
#define LG0() asm volatile("s_waitcnt lgkmcnt(0)" ::: "memory")
#define CBAR() asm volatile("" ::: "memory")
__device__ __forceinline__ void wg_barrier() { LG0(); __builtin_amdgcn_s_barrier(); CBAR(); }
__device__ __forceinline__ v4u ldh(rsrc_t rs, int off) { return __builtin_amdgcn_raw_buffer_load_b128(rs, off, 0, SC1); }
__device__ __forceinline__ void sth(rsrc_t rs, int off, v4u v) { __builtin_amdgcn_raw_buffer_store_b128(v, rs, off, 0, SC1); }
__device__ __forceinline__ unsigned lflag(lptr L, int f) { return __hip_atomic_load((LAS unsigned*)(L + L_FLAGS) + f, __ATOMIC_RELAXED, __HIP_MEMORY_SCOPE_WORKGROUP); }
__device__ __forceinline__ void lwait(lptr L, int f, unsigned want) {
#pragma unroll 1
  for (unsigned i = 0; i < LSPIN; ++i) { if ((unsigned)__builtin_amdgcn_readfirstlane(lflag(L, f)) >= want) break; __builtin_amdgcn_s_sleep(1); }
  CBAR();
}
__device__ __forceinline__ unsigned larrive(int f) {
  unsigned old;
  asm volatile("s_mov_b64 exec, 1\n\tds_add_rtn_u32 %0, %1, %2\n\ts_mov_b64 exec, -1\n\ts_waitcnt lgkmcnt(0)" : "=&v"(old) : "v"(L_FLAGS + 4 * f), "v"(1u) : "memory");
  return __builtin_amdgcn_readfirstlane(old);
}
__device__ __forceinline__ void lsignal(int f) {
  asm volatile("s_mov_b64 exec, 1\n\tds_add_u32 %0, %1\n\ts_mov_b64 exec, -1" ::"v"(L_FLAGS + 4 * f), "v"(1u) : "memory");
}
__device__ __forceinline__ v4u trfrag(lptr p, int rs4) {
  s16x4 a = __builtin_amdgcn_ds_read_tr16_b64_v4i16((LAS s16x4*)p), b = __builtin_amdgcn_ds_read_tr16_b64_v4i16((LAS s16x4*)(p + rs4));
  v2u x = __builtin_bit_cast(v2u, a), y = __builtin_bit_cast(v2u, b);
  return v4u{x.x, x.y, y.x, y.y};
}
template <int BITS> __device__ __forceinline__ v4u tagv(unsigned tag) { constexpr unsigned m = (1u << BITS) - 1u; return v4u{tag & m, (tag + 1u) & m, (tag + 2u) & m, (tag + 3u) & m}; }
template <int BITS> __device__ __forceinline__ v4u enc(f32x4 v, unsigned tag) {
  constexpr unsigned m = (1u << BITS) - 1u;
  return ((__builtin_bit_cast(v4u, v) + (1u << (BITS - 1))) & ~m) | tagv<BITS>(tag);
}
template <int BITS, int N, int SLEEP> __device__ __forceinline__ void gather(rsrc_t rs, const int (&off)[N], v4u (&v)[N], unsigned tag) {
  const v4u tv = tagv<BITS>(tag);
#pragma unroll 1
  for (unsigned i = 0; i < GSPIN; ++i) {
    CBAR();
#pragma unroll
    for (int k = 0; k < N; ++k) v[k] = ldh(rs, off[k]);
    unsigned bad = 0;
#pragma unroll
    for (int k = 0; k < N; ++k) { v[k] ^= tv; bad |= v[k].x | v[k].y | v[k].z | v[k].w; }
    if (__all((bad & ((1u << BITS) - 1u)) == 0u)) break;
    if (SLEEP) __builtin_amdgcn_s_sleep(SLEEP);
  }
}
__device__ __forceinline__ f32x4 asf(v4u v) { return __builtin_bit_cast(f32x4, v); }
__device__ __forceinline__ void make_sel(unsigned (&sel)[2][4], int lane) {
  const int jp = lane & 7;
#pragma unroll
  for (int hs = 0; hs < 2; ++hs)
#pragma unroll
    for (int d = 0; d < 4; ++d) {
      const unsigned b01 = hs ? 0x0302u : 0x0100u;
      sel[hs][d] = (d == (jp >> 1)) ? ((jp & 1) ? ((b01 << 16) | 0x0c0cu) : (0x0c0c0000u | b01)) : 0x0c0c0c0cu;
    }
}
__device__ __forceinline__ v4u diag(unsigned src, const unsigned (&s)[4]) {
  return v4u{__builtin_amdgcn_perm(src, src, s[0]), __builtin_amdgcn_perm(src, src, s[1]), __builtin_amdgcn_perm(src, src, s[2]), __builtin_amdgcn_perm(src, src, s[3])};
}
__device__ __forceinline__ void gemv_unit(lptr L, rsrc_t rsW, const v4u (&wf)[4], v4u af, int cw, int lane, int flag, int rd, int rd2, int st, int st2, unsigned tag) {
  const int G = lane >> 4, i16 = lane & 15;
  f32x4 acc[4];
#pragma unroll
  for (int t = 0; t < 4; ++t) acc[t] = mfma(wf[t], af, f32x4{0.f, 0.f, 0.f, 0.f});
  if (i16 < 8) {
#pragma unroll
    for (int t = 0; t < 4; ++t) *(LAS f32x4*)(L + L_RED + ((cw * 8 + i16) * 64 + 16 * t + 4 * G) * 4) = acc[t];
  }
  if (larrive(flag) == 3u) {
    f32x4 p[2][4];
#pragma unroll
    for (int q = 0; q < 2; ++q)
#pragma unroll
      for (int k = 0; k < 4; ++k) p[q][k] = *(LAS f32x4*)(L + L_RED + k * 2048 + (rd + q * rd2) * 16);
#pragma unroll
    for (int q = 0; q < 2; ++q) sth(rsW, st + q * st2, enc<4>((p[q][0] + p[q][1]) + (p[q][2] + p[q][3]), tag));
  }
}

template <int NS> __device__ __forceinline__ void c_load_w(lptr L, v4u (&wf)[NS][8], int k0, int lane) {
#pragma unroll
  for (int q = 0; q < NS; ++q)
#pragma unroll
    for (int i = 0; i < 8; ++i) wf[q][i] = *(LAS v4u*)(L + ((lane >> 3) & 1) * 65536 + (k0 + 32 * q + 8 * (lane >> 4) + i) * 128 + (lane & 7) * 16);
}
template <int NS> __device__ __forceinline__ void c_gate_up(lptr L, const v4u (&wf)[NS][8], const unsigned (&sel)[2][4], int k0, int slot, int lane) {
  v4u hs[NS];
#pragma unroll
  for (int q = 0; q < NS; ++q) hs[q] = *(LAS v4u*)(L + L_RED + ((lane >> 3) & 1) * 1024 + (k0 + 32 * q + 8 * (lane >> 4)) * 2);
  f32x4 acc[2] = {{0.f, 0.f, 0.f, 0.f}, {0.f, 0.f, 0.f, 0.f}};
#pragma unroll
  for (int q = 0; q < NS; ++q)
#pragma unroll
    for (int i = 0; i < 8; ++i) acc[i & 1] = mfma(diag(hs[q][i >> 1], sel[i & 1]), wf[q][i], acc[i & 1]);
  *(LAS f32x4*)(L + L_OB + slot * 1024 + lane * 16) = acc[0] + acc[1];
}

struct Args {
  const unsigned short *x, *kc, *vc, *wq, *wo, *wg, *wu, *wd, *c, *wm;
  unsigned short* out;
  unsigned char* ws;
};

#define STAMP(i) do { if ((i) == DIAG_S) dgt = (unsigned)__builtin_amdgcn_s_memrealtime(); } while (0)
#define EXIT() do { if (w >= DIAG_W0 && w < DIAG_W1 && dgt != 0u && (DIAG_ROLE == 0 || (DIAG_ROLE == 1) == (r < 16))) { const unsigned te_ = dg0 + 4000u + 4u * (dgt - dg0); \
    for (int i_ = 0; i_ < 100000 && (int)((unsigned)__builtin_amdgcn_s_memrealtime() - te_) < 0; ++i_) __builtin_amdgcn_s_sleep(1); } return; } while (0)
#define DEPTHV 10
#define DEPTHW 5
#define T_H1 380
#define T_H2 150
template <int N> __device__ __forceinline__ void vmw() { asm volatile("s_waitcnt vmcnt(%0)" ::"n"(N) : "memory"); }
__device__ __forceinline__ unsigned now() { return (unsigned)__builtin_amdgcn_s_memrealtime(); }
__device__ __forceinline__ void pause_until(unsigned t) {
#pragma unroll 1
  for (int i = 0; i < 4096 && (int)(now() - t) < 0; ++i) __builtin_amdgcn_s_sleep(2);
}

}

extern "C" __global__ void __launch_bounds__(1024, 4) decode_block(Args a) {
  extern __shared__ __attribute__((aligned(16))) unsigned char smem[];
  u64 dg0_; asm volatile("s_memrealtime %0\n\ts_waitcnt lgkmcnt(0)" : "=s"(dg0_) :: "memory");
  const unsigned dg0 = (unsigned)dg0_; unsigned dgt = 0;
  if (DIAG_S == 100) dgt = dg0;
  if (DIAG_S == 101) { asm volatile("" :: "s"(a.ws), "s"(a.x) : "memory"); dgt = (unsigned)__builtin_amdgcn_s_memrealtime(); }
  const lptr L = (lptr)smem;
  const int lane = threadIdx.x & 63, w = __builtin_amdgcn_readfirstlane(threadIdx.x >> 6);
  const int wg = blockIdx.x, xc = wg & 7, r = wg >> 3;
  const int G = lane >> 4, i16 = lane & 15;
  const size_t kvoff = ((size_t)((r >> 2) * 8 + xc) * SK + (SK - WIN) + 512 * (r & 3)) * 128;
  const rsrc_t rsW = mk(a.ws, WS_TOTAL);
  const bool isC = r >= 16 && r < 30;
  const int cidx = 14 * xc + (r - 16), fg = cidx >> 2, cb0 = 2 * (cidx & 3);
  STAMP(0);
  unsigned tag;

  if (w >= 8) {
    const int v = w - 8, j = v & 3, hf = v >> 2;
    const unsigned tv0 = now();
    if (v == 0 && lane < F_N) ((LAS unsigned*)(L + L_FLAGS))[lane] = 0u;
    if (v == 7) {
      u64 pc; unsigned rel;
      asm volatile("s_getpc_b64 %0\nLpf_here:\n\ts_mov_b32 %1, Lpf_here-decode_block" : "=s"(pc), "=s"(rel));
      dma_df(mk((const void*)(pc - rel), CODEPF * 1024), lane * 16, r * 1024, L_OB + 7 * 2048);
    }
    wg_barrier();
    const rsrc_t rsV = mk(a.vc + kvoff, 131072);
    v4u vr[16];
    if (hf == 1) {
      const v4u z = {0u, 0u, 0u, 0u};
      if (j < 2) sth(rsW, WS_OP + (xc * 32 + r) * 2048 + (j * 64 + lane) * 16, z);
      if (r < 16) { if (j == 2) sth(rsW, WS_AP + (((4 * (r >> 3) + G) * 8 + xc) * 512 + 64 * (r & 7) + 4 * i16) * 4, z); }
      else if (isC) sth(rsW, WS_FP + (((cb0 + (G >> 1)) * 16 + 4 * j + (i16 >> 2)) * 28 + fg) * 128 + ((G & 1) * 4 + (i16 & 3)) * 16, z);
    }
    if (PFC && hf == 1) {
      if (T_PF) pause_until(tv0 + T_PF);
      const int fg0 = (14 * xc) >> 2;
      if (PFC & 1) {
        const rsrc_t rsG = mk(a.wg, 512 * FF * 2), rsU = mk(a.wu, 512 * FF * 2);
#pragma unroll
        for (int i = 0; i < 2; ++i) {
          dma_df(rsG, (lane >> 5) * (FF * 2) + (lane & 31) * 16, (16 * r + 4 * j + 2 * i) * (FF * 2) + 128 * fg0, L_OB + v * 2048);
          dma_df(rsU, (lane >> 5) * (FF * 2) + (lane & 31) * 16, (16 * r + 4 * j + 2 * i) * (FF * 2) + 128 * fg0, L_OB + v * 2048 + 1024);
        }
      }
      if (PFC & 2) {
        const rsrc_t rsD = mk(a.wd, FF * 512 * 2);
#pragma unroll
        for (int i = 0; i < 2; ++i) dma_df(rsD, lane * 16, (64 * fg0 + 8 * r + 2 * j + i) * 1024, L_OB + v * 2048 + i * 1024);
      }
      if (PFC & 4) {
        const rsrc_t rsO = mk(a.wo, 4096 * 512 * 2);
#pragma unroll
        for (int i = 0; i < 4; ++i) { dma_df(rsO, lane * 16, (512 * xc + 16 * r + 4 * j + i) * 1024, L_OB + v * 2048 + (i & 1) * 1024); }
      }
    }
    lwait(L, F_KI + j, 1 + hf);
#pragma unroll
    for (int i = 0; i < 16; ++i) { vr[i] = __builtin_amdgcn_raw_buffer_load_b128(rsV, lane * 16 + j * 1024, (hf * 16 + i) * 4096, KVPOL); if (hf) vmw<DEPTHV - 1>(); else vmw<DEPTH - 1>(); }
    __builtin_amdgcn_sched_barrier(0);
    lsignal(F_KI + j);
    lsignal(F_VI);
    __builtin_amdgcn_s_setprio(3);
    STAMP(1);
    unsigned s2[2][2];
#pragma unroll
    for (int hs = 0; hs < 2; ++hs)
#pragma unroll
      for (int d = 0; d < 2; ++d) {
        const unsigned b01 = hs ? 0x0302u : 0x0100u;
        s2[hs][d] = (d == ((lane >> 1) & 1)) ? ((lane & 1) ? ((b01 << 16) | 0x0c0cu) : (0x0c0c0000u | b01)) : 0x0c0c0c0cu;
      }
    lwait(L, F_L, 4);
    STAMP(2);
    const lptr pb = L + L_PB + (((j * 2 + hf) * 4 + G) * 4 + (i16 >> 2)) * 32;
    const v4u pw[2] = {*(LAS v4u*)pb, *(LAS v4u*)(pb + 16)};
    float fs;
    {
      const LAS float* ms = (const LAS float*)(L + L_MST) + G * 2;
      const float M = fmaxf(fmaxf(ms[0], ms[8]), fmaxf(ms[16], ms[24]));
      fs = __builtin_amdgcn_exp2f((ms[j * 8] - M) * LOG2E);
    }
    tag = __builtin_amdgcn_readfirstlane(lflag(L, F_TAG));
    f32x4 acc[2] = {{0.f, 0.f, 0.f, 0.f}, {0.f, 0.f, 0.f, 0.f}};
#pragma unroll
    for (int i = 0; i < 16; ++i) {
      const unsigned src = pw[i >> 3][(i >> 1) & 3];
      const v2u A = {__builtin_amdgcn_perm(src, src, s2[i & 1][0]), __builtin_amdgcn_perm(src, src, s2[i & 1][1])};
      acc[0] = mfma16(A, v2u{vr[i].x, vr[i].y}, acc[0]);
      acc[1] = mfma16(A, v2u{vr[i].z, vr[i].w}, acc[1]);
    }
    STAMP(3);
#pragma unroll
    for (int q = 0; q < 2; ++q) *(LAS f32x4*)(L + L_OB + v * 2048 + lane * 32 + q * 16) = acc[q] * fs;
    if (larrive(F_V) == 7u) {
      f32x4 p[2][8];
#pragma unroll
      for (int q = 0; q < 2; ++q)
#pragma unroll
        for (int k = 0; k < 8; ++k) p[q][k] = *(LAS f32x4*)(L + L_OB + k * 2048 + (q * 64 + lane) * 16);
#pragma unroll
      for (int q = 0; q < 2; ++q)
        sth(rsW, WS_OP + (xc * 32 + r) * 2048 + (q * 64 + lane) * 16, enc<8>(((p[q][0] + p[q][1]) + (p[q][2] + p[q][3])) + ((p[q][4] + p[q][5]) + (p[q][6] + p[q][7])), tag));
      lsignal(F_PUB);
      STAMP(4);
    }
    if (!isC || ABL >= 1) EXIT();
    unsigned sel[2][4];
    make_sel(sel, lane);
    if (hf != 0) {
      wg_barrier();
      v4u wf[2][8];
      c_load_w<2>(L, wf, 256 + 64 * j, lane);
      wg_barrier();
      wg_barrier();
      c_gate_up<2>(L, wf, sel, 256 + 64 * j, 8 + j, lane);
      wg_barrier();
      EXIT();
    }
    lwait(L, F_PUB, 1);
    v4u dw[16];
    {
      const rsrc_t rsD = mk(a.wd, FF * 512 * 2);
#pragma unroll
      for (int i = 0; i < 16; ++i) dw[i] = __builtin_amdgcn_raw_buffer_load_b128(rsD, G * 16384 + i16 * 16 + j * 256, (64 * fg + i) * 1024, WPOL);
      __builtin_amdgcn_sched_barrier(0);
    }
    wg_barrier();
    wg_barrier();
    wg_barrier();
    wg_barrier();
    f32x4 p[12];
#pragma unroll
    for (int k = 0; k < 12; ++k) p[k] = *(LAS f32x4*)(L + L_OB + k * 1024 + lane * 16);
    const f32x4 gu = (((p[0] + p[1]) + (p[2] + p[3])) + ((p[4] + p[5]) + (p[6] + p[7]))) + ((p[8] + p[9]) + (p[10] + p[11]));
    float av[4];
#pragma unroll
    for (int d = 0; d < 4; ++d) { const float t = rb(gu[d]); av[d] = rb(silu_f(t)) * dpp<ROR8>(t); }
    const lptr ab = L + L_RED + 2048 + j * 512;
    *(LAS v2u*)(ab + (G >> 1) * 256 + (8 * i16 + 4 * (G & 1)) * 2) = v2u{pk2(av[0], av[1]), pk2(av[2], av[3])};
    CBAR();
    const v4u a0 = *(LAS v4u*)(ab + ((lane >> 3) & 1) * 256 + G * 32), a1 = *(LAS v4u*)(ab + ((lane >> 3) & 1) * 256 + G * 32 + 16);
    f32x4 fa[2] = {{0.f, 0.f, 0.f, 0.f}, {0.f, 0.f, 0.f, 0.f}};
#pragma unroll
    for (int i = 0; i < 16; ++i) fa[i & 1] = mfma(diag(i < 8 ? a0[(i >> 1) & 3] : a1[(i >> 1) & 3], sel[i & 1]), dw[i], fa[i & 1]);
    sth(rsW, WS_FP + (((cb0 + (G >> 1)) * 16 + 4 * j + (i16 >> 2)) * 28 + fg) * 128 + ((G & 1) * 4 + (i16 & 3)) * 16, enc<8>(fa[0] + fa[1], tag));
    STAMP(10);
    EXIT();
  } else if (w >= 4) {
    const int j = w - 4;
    const rsrc_t rsK = mk(a.kc + kvoff, 131072);
    unsigned voff[4];
#pragma unroll
    for (int q = 0; q < 4; ++q) voff[q] = j * 1024 + G * 256 + ((i16 ^ (4 * q + G)) << 4);
#pragma unroll
    for (int i = 0; i < DEPTH - 1; ++i) DMA_K(rsK, voff[i & 3], i * 4096, i * 4096 + j * 1024);
    wg_barrier();
#pragma unroll
    for (int i = DEPTH - 1; i < 24; ++i) { DMA_K(rsK, voff[i & 3], i * 4096, i * 4096 + j * 1024); VMP(); }
    lwait(L, F_EW, 4);
#pragma unroll
    for (int i = 24; i < 32; ++i) { DMA_K(rsK, voff[i & 3], i * 4096, i * 4096 + j * 1024); VMP(); }
    lsignal(F_KI + j);
    STAMP(1);
    VM0();
    STAMP(2);
    const lptr kb = L + j * 1024 + (i16 >> 2) * 4096 + (i16 & 3) * 256;
    v4u kf[8][4];
#define KREAD(u) _Pragma("unroll") for (int ks = 0; ks < 4; ++ks) kf[u][ks] = *(LAS v4u*)(kb + (u) * 16384 + (((4 * ks + G) ^ i16) << 4))
    KREAD(0); KREAD(4); KREAD(1); KREAD(5); KREAD(2);
    __builtin_amdgcn_sched_barrier(0);
    lwait(L, F_QR, 4);
    STAMP(3);
    const int h = i16 & 3, rho = i16 >> 2;
    v4u qf[4];
#pragma unroll
    for (int ks = 0; ks < 4; ++ks) qf[ks] = *(LAS v4u*)(L + L_QS + h * 256 + ks * 64 + G * 16);
    tag = lflag(L, F_TAG);
    f32x4 sc[2] = {{0.f, 0.f, 0.f, 0.f}, {0.f, 0.f, 0.f, 0.f}};
#define KMMA(rp) _Pragma("unroll") for (int ks = 0; ks < 4; ++ks) { const v4u qm = (rho == (rp)) ? qf[ks] : v4u{0u, 0u, 0u, 0u}; \
      sc[0] = mfma(kf[rp][ks], qm, sc[0]); sc[1] = mfma(kf[4 + (rp)][ks], qm, sc[1]); }
    __builtin_amdgcn_sched_barrier(0);
    KMMA(0);
    __builtin_amdgcn_sched_barrier(0);
    KREAD(6); KREAD(3);
    __builtin_amdgcn_sched_barrier(0);
    KMMA(1);
    __builtin_amdgcn_sched_barrier(0);
    KREAD(7);
    __builtin_amdgcn_sched_barrier(0);
    KMMA(2); KMMA(3);
    tag = __builtin_amdgcn_readfirstlane(tag);
    float s[8];
#pragma unroll
    for (int k = 0; k < 8; ++k) s[k] = rb(rb(sc[k >> 2][k & 3]) * 0.08838834764831845f);
    float m = fmaxf(fmaxf(fmaxf(s[0], s[1]), fmaxf(s[2], s[3])), fmaxf(fmaxf(s[4], s[5]), fmaxf(s[6], s[7])));
    m = fmaxf(m, dpp<ROR4>(m)); m = fmaxf(m, dpp<ROR8>(m)); m = x32_max(x16_max(m));
    float l = 0.f;
#pragma unroll
    for (int k = 0; k < 8; ++k) {
      const float p = __builtin_amdgcn_exp2f((s[k] - m) * LOG2E);
      l += p;
      *(LAS __bf16*)(L + L_PB + ((((j * 2 + (k >> 2)) * 4 + (k & 3)) * 4 + h) * 16 + 4 * rho + G) * 2) = (__bf16)p;
    }
    l += dpp<ROR4>(l); l += dpp<ROR8>(l); l = x32_sum(x16_sum(l));
    if (lane < 4) { ((LAS float*)(L + L_MST))[(j * 4 + lane) * 2] = m; ((LAS float*)(L + L_MST))[(j * 4 + lane) * 2 + 1] = l; }
    if (larrive(F_L) == 3u) {
      if (lane < 4) {
        const LAS float* ms = (const LAS float*)(L + L_MST) + lane * 2;
        const float M = fmaxf(fmaxf(ms[0], ms[8]), fmaxf(ms[16], ms[24]));
        float Ls = 0.f;
#pragma unroll
        for (int k = 0; k < 4; ++k) Ls += ms[k * 8 + 1] * __builtin_amdgcn_exp2f((ms[k * 8] - M) * LOG2E);
        sth(rsW, WS_ST + (((xc * 8 + (r >> 2)) * 4 + lane) * 4 + (r & 3)) * 16, v4u{__float_as_uint(M), tag, __float_as_uint(Ls), ~tag});
      }
    }
    STAMP(4);
  } else {
    const int cw = w;
    const unsigned tstart = now();
    const bool isM = r < 24;
    const int ksq = r & 3, cgq = r >> 2;
    const int mg = cgq < 2 ? 2 * xc + cgq : 16 + 4 * xc + (cgq - 2);
    const rsrc_t rsM = mk(a.wm, 512 * 3072 * 2), rsQ = mk(a.wq, 512 * 4096 * 2), rsC = mk(a.c, 8192), rsX = mk(a.x, 8192);
    const int krow = 128 * ksq + 32 * cw;
    const unsigned ep = __builtin_amdgcn_raw_buffer_load_b32(rsW, WS_EPOCH, 0, SC1);
    const v4u cf = __builtin_amdgcn_raw_buffer_load_b128(rsC, (lane & 7) * 1024 + (krow + 8 * G) * 2, 0, 0);
    v4u xr[2];
#pragma unroll
    for (int i = 0; i < 2; ++i) xr[i] = __builtin_amdgcn_raw_buffer_load_b128(rsX, (2 * cw + i) * 1024 + lane * 16, 0, 0);
    const v4u xf = __builtin_amdgcn_raw_buffer_load_b128(rsX, (lane & 7) * 1024 + (krow + 8 * G) * 2, 0, 0);
    __builtin_amdgcn_sched_barrier(0);
    const bool crit = cgq < 2;
#define DMA_M() _Pragma("unroll") for (int i = 0; i < 4; ++i) dma_nt(rsM, (lane >> 3) * 6144 + (lane & 7) * 16, (krow + 8 * i) * 6144 + 128 * mg, L_EM + cw * 4096 + i * 1024)
#define DMA_Q() _Pragma("unroll") for (int i = 0; i < 4; ++i) dma_nt(rsQ, (lane >> 3) * 8192 + (lane & 7) * 16, (krow + 8 * i) * 8192 + (512 * xc + 64 * cgq) * 2, L_EQ + cw * 4096 + i * 1024)
    if (isM) DMA_M();
    DMA_Q();
    wg_barrier();
    tag = __builtin_amdgcn_readfirstlane(ep) + 1u;
    v4u sf;
#pragma unroll
    for (int d = 0; d < 4; ++d) sf[d] = pk2(silu_f(bflo(cf[d])), silu_f(bfhi(cf[d])));
    asm volatile("" : "+v"(sf));
    asm volatile("s_waitcnt vmcnt(4)" ::: "memory");
    STAMP(1);
    if (cw == 0 && lane == 0) ((LAS unsigned*)(L + L_FLAGS))[F_TAG] = tag;
    const int troff = cw * 4096 + (8 * G + (i16 >> 2)) * 128 + (i16 & 3) * 8;
    if (isM) {
      v4u wmf[4];
#pragma unroll
      for (int t = 0; t < 4; ++t) wmf[t] = trfrag(L + L_EM + troff + t * 32, 4 * 128);
      const int rd = crit ? (lane & 7) * 16 + (lane >> 3) : lane, rd2 = crit ? 8 : 64;
      const int st = crit ? WS_MC + (ksq * 16 + 2 * (mg & 7)) * 2048 + ((lane >> 3) & 1) * 1024 + (lane >> 4) * 256 + (mg >> 3) * 128 + (lane & 7) * 16
                          : WS_MP + ((ksq * 8 + G) * 2048 + 64 * (mg - 16) + 4 * i16) * 4;
      gemv_unit(L, rsW, wmf, sf, cw, lane, F_M, rd, rd2, st, crit ? 2048 : 4 * 2048 * 4, tag);
    }
    STAMP(2);
    VM0();
    v4u wqf[4];
#pragma unroll
    for (int t = 0; t < 4; ++t) wqf[t] = trfrag(L + L_EQ + troff + t * 32, 4 * 128);
    LG0();
    asm volatile("" : "+v"(wqf[0]), "+v"(wqf[1]), "+v"(wqf[2]), "+v"(wqf[3]));
    lsignal(F_EW);
#pragma unroll
    for (int i = 0; i < 2; ++i) {
      float e[8];
#pragma unroll
      for (int d = 0; d < 4; ++d) { e[2 * d] = bflo(xr[i][d]); e[2 * d + 1] = bfhi(xr[i][d]); }
      const float mean = wave_sum(((e[0] + e[1]) + (e[2] + e[3])) + ((e[4] + e[5]) + (e[6] + e[7]))) * (1.f / 512.f);
      float q = 0.f;
#pragma unroll
      for (int d = 0; d < 8; ++d) q += (e[d] - mean) * (e[d] - mean);
      const float var = wave_sum(q) * (1.f / 512.f);
      if (lane == 0) { ((LAS float*)(L + L_STAT))[(2 * cw + i) * 2] = rb(mean); ((LAS float*)(L + L_STAT))[(2 * cw + i) * 2 + 1] = rb(__builtin_amdgcn_sqrtf(rb(rb(var) + 1e-5f))); }
    }
    lsignal(F_LN);
    lwait(L, F_LN, 4);
    float nx[8];
    {
      const float mu = ((LAS float*)(L + L_STAT))[(lane & 7) * 2], rd = __builtin_amdgcn_rcpf(((LAS float*)(L + L_STAT))[(lane & 7) * 2 + 1]);
#pragma unroll
      for (int d = 0; d < 4; ++d) { nx[2 * d] = rb(rb(bflo(xf[d]) - mu) * rd); nx[2 * d + 1] = rb(rb(bfhi(xf[d]) - mu) * rd); }
    }
    {
      v4u g[8];
      const int o0 = WS_MC + (4 * ksq + cw) * 2048 + lane * 16;
      constexpr int PS = 16 * 2048;
      const int off[8] = {o0, o0 + 1024, o0 + PS, o0 + PS + 1024, o0 + 2 * PS, o0 + 2 * PS + 1024, o0 + 3 * PS, o0 + 3 * PS + 1024};
      pause_until(tstart + T_H1);
      if (ABL < 2) gather<4, 8, 16>(rsW, off, g, tag);
      STAMP(3);
      const f32x4 m0 = (asf(g[0]) + asf(g[2])) + (asf(g[4]) + asf(g[6])), m1 = (asf(g[1]) + asf(g[3])) + (asf(g[5]) + asf(g[7]));
      float hv[8];
#pragma unroll
      for (int d = 0; d < 8; ++d) {
        const float sa = rb(d < 4 ? m0[d & 3] : m1[d & 3]), ca = dpp<ROR8>(sa);
        hv[d] = rb(nx[d] * rb(1.f + ca)) + sa;
      }
      const v4u hf = {pk2(hv[0], hv[1]), pk2(hv[2], hv[3]), pk2(hv[4], hv[5]), pk2(hv[6], hv[7])};
      gemv_unit(L, rsW, wqf, hf, cw, lane, F_Q, lane, 64, WS_QP + ((ksq * 8 + G) * 4096 + 512 * xc + 64 * cgq + 4 * i16) * 4, 4 * 4096 * 4, tag);
    }
    STAMP(4);
    {
      v4u g[2];
      const int o0 = WS_QP + (((lane >> 5) * 8 + (r >> 2)) * 4096 + 512 * xc + 128 * cw + 4 * (lane & 31)) * 4;
      const int off[2] = {o0, o0 + 2 * 8 * 4096 * 4};
      pause_until(now() + T_H2);
      if (ABL < 2) gather<4, 2, 16>(rsW, off, g, tag);
      const f32x4 s = asf(g[0]) + asf(g[1]);
      const float q0 = x32_sum(s.x), q1 = x32_sum(s.y), q2 = x32_sum(s.z), q3 = x32_sum(s.w);
      if (lane < 32) *(LAS v2u*)(L + L_QS + cw * 256 + lane * 8) = v2u{pk2(q0, q1), pk2(q2, q3)};
      lsignal(F_QR);
    }
    STAMP(5);
    if (cw == 0 && ABL < 2) {
      v4u g[1];
      const int off[1] = {WS_MP + ((G * 8 + xc) * 2048 + 64 * r + 4 * i16) * 4};
      gather<4, 1, 8>(rsW, off, g, tag);
      const f32x4 s = asf(g[0]);
      f32x4 o;
#pragma unroll
      for (int d = 0; d < 4; ++d) o[d] = rb(x32_sum(x16_sum(s[d])));
      if (lane < 16) sth(rsW, WS_MOD + (xc * 2048 + 64 * r + 4 * i16) * 4, __builtin_bit_cast(v4u, o) | tagv<16>(tag));
    }
  }

  if (r >= 30 || ABL >= 1) EXIT();
  const int tw = w;
  lwait(L, F_L, 4);
  lwait(L, F_VI, 8);
  STAMP(6);
  if (r < 16) {
    const int cg = r & 7, bg = r >> 3;
    {
      const rsrc_t rsO = mk(a.wo, 4096 * 512 * 2);
#pragma unroll
      for (int i = 0; i < 8; ++i) DMA_W(rsO, (lane >> 3) * 1024 + (lane & 7) * 16, (512 * xc + 64 * tw + 8 * i) * 1024 + 128 * cg, tw * 8192 + i * 1024), vmw<DEPTHW - 1>();
    }
    STAMP(14);
    float scl[4];
    {
      v4u g;
#pragma unroll 1
      for (unsigned i = 0; i < GSPIN; ++i) {
        CBAR();
        g = ldh(rsW, WS_ST + (((xc * 8 + 4 * bg + G) * 4 + (tw >> 1)) * 4 + (lane & 3)) * 16);
        if (__all(g.y == tag && g.w == ~tag)) break;
        __builtin_amdgcn_s_sleep(2);
      }
      const float ms = __uint_as_float(g.x), ls = __uint_as_float(g.z);
      float M = fmaxf(ms, dpp<XOR1>(ms)); M = fmaxf(M, dpp<XOR2>(M));
      const float e = __builtin_amdgcn_exp2f((ms - M) * LOG2E);
      float den = e * ls; den += dpp<XOR1>(den); den += dpp<XOR2>(den);
      const float sc = e * __builtin_amdgcn_rcpf(den);
      scl[0] = dpp<0x00>(sc); scl[1] = dpp<0x55>(sc); scl[2] = dpp<0xAA>(sc); scl[3] = dpp<0xFF>(sc);
    }
    STAMP(15);
    VM0();
    v4u wf[2][4];
#pragma unroll
    for (int kk = 0; kk < 2; ++kk)
#pragma unroll
      for (int t = 0; t < 4; ++t) wf[kk][t] = trfrag(L + tw * 8192 + (32 * kk + 8 * G + (i16 >> 2)) * 128 + t * 32 + (i16 & 3) * 8, 4 * 128);
    STAMP(7);
    lwait(L, F_PUB, 1);
    STAMP(8);
    {
      v4u g[4];
      const int o0 = WS_OP + (xc * 32 + (4 * bg + G) * 4) * 2048 + tw * 256 + i16 * 16;
      const int off[4] = {o0, o0 + 2048, o0 + 4096, o0 + 6144};
      gather<8, 4, 0>(rsW, off, g, tag);
      STAMP(9);
      const f32x4 o = (asf(g[0]) * scl[0] + asf(g[1]) * scl[1]) + (asf(g[2]) * scl[2] + asf(g[3]) * scl[3]);
      const lptr ob = L + 65536 + tw * 512;
      *(LAS v2u*)(ob + lane * 8) = v2u{pk2(o.x, o.y), pk2(o.z, o.w)};
      CBAR();
      const v4u of0 = *(LAS v4u*)(ob + (lane & 3) * 128 + G * 16), of1 = *(LAS v4u*)(ob + (lane & 3) * 128 + 64 + G * 16);
      f32x4 acc[4];
#pragma unroll
      for (int t = 0; t < 4; ++t) acc[t] = mfma(wf[0][t], of0, f32x4{0.f, 0.f, 0.f, 0.f});
#pragma unroll
      for (int t = 0; t < 4; ++t) acc[t] = mfma(wf[1][t], of1, acc[t]);
      const int rep = i16 >> 2;
      const f32x4 d = rep == 0 ? acc[0] : rep == 1 ? acc[1] : rep == 2 ? acc[2] : acc[3];
      *(LAS f32x4*)(L + L_RED + tw * 1024 + (lane & 3) * 256 + rep * 64 + G * 16) = d;
    }
    wg_barrier();
    if (tw == 0) {
      f32x4 p[8];
#pragma unroll
      for (int k = 0; k < 8; ++k) p[k] = *(LAS f32x4*)(L + L_RED + k * 1024 + lane * 16);
      sth(rsW, WS_AP + (((4 * bg + G) * 8 + xc) * 512 + 64 * cg + 4 * i16) * 4, enc<8>(((p[0] + p[1]) + (p[2] + p[3])) + ((p[4] + p[5]) + (p[6] + p[7])), tag));
      STAMP(10);
    }
    if (tw != 1) EXIT();
    const int col = 32 * r + 8 * (lane & 3) + (lane & 4), pg = lane >> 3;
    const v2u xx = __builtin_amdgcn_raw_buffer_load_b64(mk(a.x, 8192), (xc * 512 + col) * 2, 0, 0);
    v4u md[2];
    {
      const int off[2] = {WS_MOD + (xc * 2048 + col) * 4, WS_MOD + (xc * 2048 + 1536 + col) * 4};
      gather<16, 2, 2>(rsW, off, md, tag);
    }
    f32x4 x2;
    {
      v4u p[1];
      const int off1[1] = {WS_AP + ((xc * 8 + pg) * 512 + col) * 4};
      gather<8, 1, 2>(rsW, off1, p, tag);
      f32x4 s = asf(p[0]);
#pragma unroll
      for (int d = 0; d < 4; ++d) { float t = s[d]; t += dpp<ROR8>(t); s[d] = x32_sum(x16_sum(t)); }
      const f32x4 ga = asf(md[0]);
      x2.x = rb(bflo(xx.x) + rb(ga.x * rb(s.x))); x2.y = rb(bfhi(xx.x) + rb(ga.y * rb(s.y)));
      x2.z = rb(bflo(xx.y) + rb(ga.z * rb(s.z))); x2.w = rb(bfhi(xx.y) + rb(ga.w * rb(s.w)));
    }
    STAMP(11);
    {
      v4u g[4];
      const int o0 = WS_FP + (xc * 16 + r) * 3584 + lane * 16;
      const int off[4] = {o0, o0 + 1024, o0 + 2048, lane < 32 ? o0 + 3072 : o0};
      gather<8, 4, 0>(rsW, off, g, tag);
      STAMP(12);
      f32x4 s = (asf(g[0]) + asf(g[1])) + asf(g[2]);
      if (lane < 32) s += asf(g[3]);
#pragma unroll
      for (int d = 0; d < 4; ++d) { float t = s[d]; t += dpp<ROR8>(t); s[d] = x32_sum(x16_sum(t)); }
      const f32x4 gb = asf(md[1]);
      const float o0f = x2.x + rb(gb.x * rb(s.x)), o1 = x2.y + rb(gb.y * rb(s.y)), o2 = x2.z + rb(gb.z * rb(s.z)), o3 = x2.w + rb(gb.w * rb(s.w));
      if (lane < 8) __builtin_amdgcn_raw_buffer_store_b64(v2u{pk2(o0f, o1), pk2(o2, o3)}, mk(a.out, 8192), (xc * 512 + col) * 2, 0, 0);
    }
    STAMP(13);
  } else {
    const int b = cb0 + (tw >> 2), col = 128 * (tw & 3) + 4 * (lane & 31);
    {
      const rsrc_t rsG = mk(a.wg, 512 * FF * 2), rsU = mk(a.wu, 512 * FF * 2);
      const int rot = 0;
#pragma unroll
      for (int i = 0; i < 8; ++i) { const int p = (i + rot) & 7;
        DMA_W(rsG, (lane >> 3) * (FF * 2) + (lane & 7) * 16, (64 * tw + 8 * p) * (FF * 2) + 128 * fg, tw * 8192 + p * 1024); vmw<DEPTHW - 1>();
        DMA_W(rsU, (lane >> 3) * (FF * 2) + (lane & 7) * 16, (64 * tw + 8 * p) * (FF * 2) + 128 * fg, 65536 + tw * 8192 + p * 1024); vmw<DEPTHW - 1>(); }
    }
    STAMP(11);
    unsigned sel[2][4];
    make_sel(sel, lane);
    const v2u xx = __builtin_amdgcn_raw_buffer_load_b64(mk(a.x, 8192), (b * 512 + col) * 2, 0, 0);
    v4u md[3];
    {
      const int o0 = WS_MOD + (b * 2048 + col) * 4;
      const int off[3] = {o0, o0 + 512 * 4, o0 + 1024 * 4};
      gather<16, 3, 2>(rsW, off, md, tag);
    }
    VM0();
    STAMP(12);
    wg_barrier();
    v4u wf[1][8];
    c_load_w<1>(L, wf, 32 * tw, lane);
    STAMP(7);
    const f32x4 ga = asf(md[0]), sb = asf(md[1]);
    f32x4 cb1 = asf(md[2]);
#pragma unroll
    for (int d = 0; d < 4; ++d) cb1[d] = rb(1.f + cb1[d]);
    float x2[4];
    {
      v4u g[4];
      const int o0 = WS_AP + ((b * 8 + 4 * (lane >> 5)) * 512 + col) * 4;
      const int off[4] = {o0, o0 + 2048, o0 + 4096, o0 + 6144};
      gather<8, 4, 0>(rsW, off, g, tag);
      STAMP(9);
      const f32x4 s = (asf(g[0]) + asf(g[1])) + (asf(g[2]) + asf(g[3]));
      x2[0] = rb(bflo(xx.x) + rb(ga.x * rb(x32_sum(s.x)))); x2[1] = rb(bfhi(xx.x) + rb(ga.y * rb(x32_sum(s.y))));
      x2[2] = rb(bflo(xx.y) + rb(ga.z * rb(x32_sum(s.z)))); x2[3] = rb(bfhi(xx.y) + rb(ga.w * rb(x32_sum(s.w))));
    }
    if (wg == 128 && tw == 0 && lane == 0) __builtin_amdgcn_raw_buffer_store_b32(tag, rsW, WS_EPOCH, 0, SC1);
    const float c0 = __builtin_bit_cast(float, __builtin_amdgcn_readfirstlane(__builtin_bit_cast(int, x2[0])));
    const float d0 = x2[0] - c0, d1 = x2[1] - c0, d2 = x2[2] - c0, d3 = x2[3] - c0;
    const float s1 = half_sum((d0 + d1) + (d2 + d3)), s2 = half_sum((d0 * d0 + d1 * d1) + (d2 * d2 + d3 * d3));
    if (lane == 0) { ((LAS float*)(L + L_STAT))[tw * 2] = c0 + s1 * (1.f / 128.f); ((LAS float*)(L + L_STAT))[tw * 2 + 1] = s2 - s1 * s1 * (1.f / 128.f); }
    wg_barrier();
    const f32x4 sa = *(LAS f32x4*)(L + L_STAT + (tw >> 2) * 32), sc = *(LAS f32x4*)(L + L_STAT + (tw >> 2) * 32 + 16);
    const float mean = 0.25f * ((sa.x + sa.z) + (sc.x + sc.z));
    const float e0 = sa.x - mean, e1 = sa.z - mean, e2 = sc.x - mean, e3 = sc.z - mean;
    const float var = fmaxf((((sa.y + sa.w) + (sc.y + sc.w)) + 128.f * ((e0 * e0 + e1 * e1) + (e2 * e2 + e3 * e3))) * (1.f / 512.f), 0.f);
    const float mu = rb(mean), rd = __builtin_amdgcn_rcpf(rb(__builtin_amdgcn_sqrtf(rb(rb(var) + 1e-5f))));
    float h2[4];
#pragma unroll
    for (int d = 0; d < 4; ++d) h2[d] = rb(rb(rb(x2[d] - mu) * rd) * cb1[d]) + sb[d];
    if (lane < 32) *(LAS v2u*)(L + L_RED + (tw >> 2) * 1024 + col * 2) = v2u{pk2(h2[0], h2[1]), pk2(h2[2], h2[3])};
    wg_barrier();
    c_gate_up<1>(L, wf, sel, 32 * tw, tw, lane);
    wg_barrier();
    STAMP(8);
  }
  EXIT();
}

extern "C" void kernel_launch(void* const* d_in, const int* in_sizes, int n_in, void* d_out, int out_size, void* d_ws, size_t ws_size, hipStream_t stream) {
  Args a;
  a.x = (const unsigned short*)d_in[0]; a.kc = (const unsigned short*)d_in[1]; a.vc = (const unsigned short*)d_in[2];
  a.wq = (const unsigned short*)d_in[3]; a.wo = (const unsigned short*)d_in[4];
  a.wg = (const unsigned short*)d_in[6]; a.wu = (const unsigned short*)d_in[7]; a.wd = (const unsigned short*)d_in[8];
  a.c = (const unsigned short*)d_in[9]; a.wm = (const unsigned short*)d_in[10];
  a.out = (unsigned short*)d_out; a.ws = (unsigned char*)d_ws;
  (void)hipFuncSetAttribute((const void*)decode_block, hipFuncAttributeMaxDynamicSharedMemorySize, L_TOTAL);
  hipLaunchKernelGGL(decode_block, dim3(256), dim3(1024), L_TOTAL, stream, a);
}
```

```cpp
#define PFC 0
#include <hip/hip_runtime.h>
#include <hip/hip_bf16.h>
#include <stdint.h>

#define STAMPS 0
#define DEPTH 6
#define T_PF 0
#define ABL 0
#define PREW 1
#define CODEPF 20

namespace {

typedef unsigned v4u __attribute__((ext_vector_type(4)));
typedef unsigned v2u __attribute__((ext_vector_type(2)));
typedef float f32x4 __attribute__((ext_vector_type(4)));
typedef short s16x4 __attribute__((ext_vector_type(4)));
typedef __bf16 bf16x8 __attribute__((ext_vector_type(8)));
typedef __bf16 bf16x2 __attribute__((ext_vector_type(2)));
typedef unsigned long long u64;
typedef __amdgpu_buffer_rsrc_t rsrc_t;
#define LAS __attribute__((address_space(3)))
typedef LAS unsigned char* lptr;

constexpr int SK = 8192, WIN = 2048, FF = 1792;
constexpr int SC1 = 16, NT = 2;
constexpr int WS_EPOCH = 0;
constexpr int WS_MC = 4096;
constexpr int WS_MP = WS_MC + 131072;
constexpr int WS_MOD = WS_MP + 262144;
constexpr int WS_QP = WS_MOD + 65536;
constexpr int WS_OP = WS_QP + 524288;
constexpr int WS_ST = WS_OP + 524288;
constexpr int WS_AP = WS_ST + 16384;
constexpr int WS_FP = WS_AP + 131072;
constexpr int WS_TOTAL = WS_FP + 458752;
constexpr int WS_STAMP = 4 << 20;
constexpr int L_EM = 98304, L_EQ = 114688;
constexpr int L_FLAGS = 131072, L_QS = L_FLAGS + 256, L_MST = L_QS + 1024, L_STAT = L_MST + 128, L_PB = L_STAT + 128, L_OB = L_PB + 4096, L_RED = L_OB + 16384,
              L_TOTAL = L_RED + 8192;
static_assert(L_TOTAL <= 163840, "LDS");
enum { F_EW = 0, F_M = 1, F_LN = 2, F_Q = 3, F_QR = 4, F_L = 5, F_VI = 6, F_V = 7, F_PUB = 8, F_TAG = 9, F_KI = 10, F_N = 16 };
constexpr unsigned GSPIN = 1u << 16, LSPIN = 1u << 20;
constexpr float LOG2E = 1.4426950408889634f;

__device__ __forceinline__ float bflo(unsigned u) { return __uint_as_float(u << 16); }
__device__ __forceinline__ float bfhi(unsigned u) { return __uint_as_float(u & 0xffff0000u); }
__device__ __forceinline__ unsigned pk2(float a, float b) { bf16x2 p = {(__bf16)a, (__bf16)b}; return __builtin_bit_cast(unsigned, p); }
__device__ __forceinline__ float rb(float a) { unsigned u = pk2(a, a); asm("" : "+v"(u)); return bfhi(u); }
template <int CTRL> __device__ __forceinline__ float dpp(float x) { return __builtin_bit_cast(float, __builtin_amdgcn_mov_dpp(__builtin_bit_cast(int, x), CTRL, 0xf, 0xf, true)); }
constexpr int XOR1 = 0xB1, XOR2 = 0x4E, HMIR = 0x141, ROR8 = 0x128, ROR4 = 0x124;
__device__ __forceinline__ float x16_sum(float x) { auto s = __builtin_amdgcn_permlane16_swap(__float_as_uint(x), __float_as_uint(x), false, false); return __uint_as_float(s[0]) + __uint_as_float(s[1]); }
__device__ __forceinline__ float x32_sum(float x) { auto s = __builtin_amdgcn_permlane32_swap(__float_as_uint(x), __float_as_uint(x), false, false); return __uint_as_float(s[0]) + __uint_as_float(s[1]); }
__device__ __forceinline__ float x16_max(float x) { auto s = __builtin_amdgcn_permlane16_swap(__float_as_uint(x), __float_as_uint(x), false, false); return fmaxf(__uint_as_float(s[0]), __uint_as_float(s[1])); }
__device__ __forceinline__ float x32_max(float x) { auto s = __builtin_amdgcn_permlane32_swap(__float_as_uint(x), __float_as_uint(x), false, false); return fmaxf(__uint_as_float(s[0]), __uint_as_float(s[1])); }
__device__ __forceinline__ float half_sum(float x) { x += dpp<XOR1>(x); x += dpp<XOR2>(x); x += dpp<HMIR>(x); x += dpp<ROR8>(x); return x16_sum(x); }
__device__ __forceinline__ float wave_sum(float x) { return x32_sum(half_sum(x)); }
__device__ __forceinline__ f32x4 mfma(v4u a, v4u b, f32x4 c) { return __builtin_amdgcn_mfma_f32_16x16x32_bf16(__builtin_bit_cast(bf16x8, a), __builtin_bit_cast(bf16x8, b), c, 0, 0, 0); }
__device__ __forceinline__ f32x4 mfma16(v2u a, v2u b, f32x4 c) { return __builtin_amdgcn_mfma_f32_16x16x16bf16_1k(__builtin_bit_cast(s16x4, a), __builtin_bit_cast(s16x4, b), c, 0, 0, 0); }
__device__ __forceinline__ float silu_f(float v) { return v * __builtin_amdgcn_rcpf(1.f + __builtin_amdgcn_exp2f(-v * LOG2E)); }
__device__ __forceinline__ rsrc_t mk(const void* p, int bytes) { return __builtin_amdgcn_make_buffer_rsrc((void*)p, 0, bytes, 0x00020000); }
__device__ __forceinline__ void dma_nt(rsrc_t rs, unsigned voff, unsigned soff, unsigned ldsaddr) {
  asm volatile("s_mov_b32 m0, %0\n\ts_nop 3\n\tbuffer_load_dwordx4 %1, %2, %3 offen nt lds" ::"s"(ldsaddr), "v"(voff), "s"(rs), "s"(soff) : "memory");
}
__device__ __forceinline__ void dma_df(rsrc_t rs, unsigned voff, unsigned soff, unsigned ldsaddr) {
  asm volatile("s_mov_b32 m0, %0\n\ts_nop 3\n\tbuffer_load_dwordx4 %1, %2, %3 offen lds" ::"s"(ldsaddr), "v"(voff), "s"(rs), "s"(soff) : "memory");
}
#define WPOL 2
#define DMA_W dma_nt
#define KVPOL 2
#define DMA_K dma_nt
#define VMP() asm volatile("s_waitcnt vmcnt(%0)" ::"n"(DEPTH - 1) : "memory")
#define VM0() asm volatile("s_waitcnt vmcnt(0)" ::: "memory")
#define LG0() asm volatile("s_waitcnt lgkmcnt(0)" ::: "memory")
#define CBAR() asm volatile("" ::: "memory")
__device__ __forceinline__ void wg_barrier() { LG0(); __builtin_amdgcn_s_barrier(); CBAR(); }
__device__ __forceinline__ v4u ldh(rsrc_t rs, int off) { return __builtin_amdgcn_raw_buffer_load_b128(rs, off, 0, SC1); }
__device__ __forceinline__ void sth(rsrc_t rs, int off, v4u v) { __builtin_amdgcn_raw_buffer_store_b128(v, rs, off, 0, SC1); }
__device__ __forceinline__ unsigned lflag(lptr L, int f) { return __hip_atomic_load((LAS unsigned*)(L + L_FLAGS) + f, __ATOMIC_RELAXED, __HIP_MEMORY_SCOPE_WORKGROUP); }
__device__ __forceinline__ void lwait(lptr L, int f, unsigned want) {
#pragma unroll 1
  for (unsigned i = 0; i < LSPIN; ++i) { if ((unsigned)__builtin_amdgcn_readfirstlane(lflag(L, f)) >= want) break; __builtin_amdgcn_s_sleep(1); }
  CBAR();
}
__device__ __forceinline__ unsigned larrive(int f) {
  unsigned old;
  asm volatile("s_mov_b64 exec, 1\n\tds_add_rtn_u32 %0, %1, %2\n\ts_mov_b64 exec, -1\n\ts_waitcnt lgkmcnt(0)" : "=&v"(old) : "v"(L_FLAGS + 4 * f), "v"(1u) : "memory");
  return __builtin_amdgcn_readfirstlane(old);
}
__device__ __forceinline__ void lsignal(int f) {
  asm volatile("s_mov_b64 exec, 1\n\tds_add_u32 %0, %1\n\ts_mov_b64 exec, -1" ::"v"(L_FLAGS + 4 * f), "v"(1u) : "memory");
}
__device__ __forceinline__ v4u trfrag(lptr p, int rs4) {
  s16x4 a = __builtin_amdgcn_ds_read_tr16_b64_v4i16((LAS s16x4*)p), b = __builtin_amdgcn_ds_read_tr16_b64_v4i16((LAS s16x4*)(p + rs4));
  v2u x = __builtin_bit_cast(v2u, a), y = __builtin_bit_cast(v2u, b);
  return v4u{x.x, x.y, y.x, y.y};
}
template <int BITS> __device__ __forceinline__ v4u tagv(unsigned tag) { constexpr unsigned m = (1u << BITS) - 1u; return v4u{tag & m, (tag + 1u) & m, (tag + 2u) & m, (tag + 3u) & m}; }
template <int BITS> __device__ __forceinline__ v4u enc(f32x4 v, unsigned tag) {
  constexpr unsigned m = (1u << BITS) - 1u;
  return ((__builtin_bit_cast(v4u, v) + (1u << (BITS - 1))) & ~m) | tagv<BITS>(tag);
}
template <int BITS, int N, int SLEEP> __device__ __forceinline__ void gather(rsrc_t rs, const int (&off)[N], v4u (&v)[N], unsigned tag) {
  const v4u tv = tagv<BITS>(tag);
#pragma unroll 1
  for (unsigned i = 0; i < GSPIN; ++i) {
    CBAR();
#pragma unroll
    for (int k = 0; k < N; ++k) v[k] = ldh(rs, off[k]);
    unsigned bad = 0;
#pragma unroll
    for (int k = 0; k < N; ++k) { v[k] ^= tv; bad |= v[k].x | v[k].y | v[k].z | v[k].w; }
    if (__all((bad & ((1u << BITS) - 1u)) == 0u)) break;
    if (SLEEP) __builtin_amdgcn_s_sleep(SLEEP);
  }
}
__device__ __forceinline__ f32x4 asf(v4u v) { return __builtin_bit_cast(f32x4, v); }
__device__ __forceinline__ void make_sel(unsigned (&sel)[2][4], int lane) {
  const int jp = lane & 7;
#pragma unroll
  for (int hs = 0; hs < 2; ++hs)
#pragma unroll
    for (int d = 0; d < 4; ++d) {
      const unsigned b01 = hs ? 0x0302u : 0x0100u;
      sel[hs][d] = (d == (jp >> 1)) ? ((jp & 1) ? ((b01 << 16) | 0x0c0cu) : (0x0c0c0000u | b01)) : 0x0c0c0c0cu;
    }
}
__device__ __forceinline__ v4u diag(unsigned src, const unsigned (&s)[4]) {
  return v4u{__builtin_amdgcn_perm(src, src, s[0]), __builtin_amdgcn_perm(src, src, s[1]), __builtin_amdgcn_perm(src, src, s[2]), __builtin_amdgcn_perm(src, src, s[3])};
}
__device__ __forceinline__ void gemv_unit(lptr L, rsrc_t rsW, const v4u (&wf)[4], v4u af, int cw, int lane, int flag, int rd, int rd2, int st, int st2, unsigned tag) {
  const int G = lane >> 4, i16 = lane & 15;
  f32x4 acc[4];
#pragma unroll
  for (int t = 0; t < 4; ++t) acc[t] = mfma(wf[t], af, f32x4{0.f, 0.f, 0.f, 0.f});
  if (i16 < 8) {
#pragma unroll
    for (int t = 0; t < 4; ++t) *(LAS f32x4*)(L + L_RED + ((cw * 8 + i16) * 64 + 16 * t + 4 * G) * 4) = acc[t];
  }
  if (larrive(flag) == 3u) {
    f32x4 p[2][4];
#pragma unroll
    for (int q = 0; q < 2; ++q)
#pragma unroll
      for (int k = 0; k < 4; ++k) p[q][k] = *(LAS f32x4*)(L + L_RED + k * 2048 + (rd + q * rd2) * 16);
#pragma unroll
    for (int q = 0; q < 2; ++q) sth(rsW, st + q * st2, enc<4>((p[q][0] + p[q][1]) + (p[q][2] + p[q][3]), tag));
  }
}

template <int NS> __device__ __forceinline__ void c_load_w(lptr L, v4u (&wf)[NS][8], int k0, int lane) {
#pragma unroll
  for (int q = 0; q < NS; ++q)
#pragma unroll
    for (int i = 0; i < 8; ++i) wf[q][i] = *(LAS v4u*)(L + ((lane >> 3) & 1) * 65536 + (k0 + 32 * q + 8 * (lane >> 4) + i) * 128 + (lane & 7) * 16);
}
template <int NS> __device__ __forceinline__ void c_gate_up(lptr L, const v4u (&wf)[NS][8], const unsigned (&sel)[2][4], int k0, int slot, int lane) {
  v4u hs[NS];
#pragma unroll
  for (int q = 0; q < NS; ++q) hs[q] = *(LAS v4u*)(L + L_RED + ((lane >> 3) & 1) * 1024 + (k0 + 32 * q + 8 * (lane >> 4)) * 2);
  f32x4 acc[2] = {{0.f, 0.f, 0.f, 0.f}, {0.f, 0.f, 0.f, 0.f}};
#pragma unroll
  for (int q = 0; q < NS; ++q)
#pragma unroll
    for (int i = 0; i < 8; ++i) acc[i & 1] = mfma(diag(hs[q][i >> 1], sel[i & 1]), wf[q][i], acc[i & 1]);
  *(LAS f32x4*)(L + L_OB + slot * 1024 + lane * 16) = acc[0] + acc[1];
}

struct Args {
  const unsigned short *x, *kc, *vc, *wq, *wo, *wg, *wu, *wd, *c, *wm;
  unsigned short* out;
  unsigned char* ws;
};

#define STAMP(i) do {} while (0)
#define EXIT() return
#define DEPTHV 10
#define DEPTHW 5
#define T_H1 380
#define T_H2 150
template <int N> __device__ __forceinline__ void vmw() { asm volatile("s_waitcnt vmcnt(%0)" ::"n"(N) : "memory"); }
__device__ __forceinline__ unsigned now() { return (unsigned)__builtin_amdgcn_s_memrealtime(); }
__device__ __forceinline__ void pause_until(unsigned t) {
#pragma unroll 1
  for (int i = 0; i < 4096 && (int)(now() - t) < 0; ++i) __builtin_amdgcn_s_sleep(2);
}

}

extern "C" __global__ void __launch_bounds__(64) dummy_k(unsigned* p) { if (threadIdx.x == 0) p[0] = (unsigned)__builtin_amdgcn_s_memrealtime(); }
extern "C" __global__ void __launch_bounds__(1024, 4) decode_block(Args a) {
  extern __shared__ __attribute__((aligned(16))) unsigned char smem[];
  const lptr L = (lptr)smem;
  const int lane = threadIdx.x & 63, w = __builtin_amdgcn_readfirstlane(threadIdx.x >> 6);
  const int wg = blockIdx.x, xc = wg & 7, r = wg >> 3;
  const int G = lane >> 4, i16 = lane & 15;
  const size_t kvoff = ((size_t)((r >> 2) * 8 + xc) * SK + (SK - WIN) + 512 * (r & 3)) * 128;
  const rsrc_t rsW = mk(a.ws, WS_TOTAL);
  const bool isC = r >= 16 && r < 30;
  const int cidx = 14 * xc + (r - 16), fg = cidx >> 2, cb0 = 2 * (cidx & 3);
  STAMP(0);
  unsigned tag;

  if (w >= 8) {
    const int v = w - 8, j = v & 3, hf = v >> 2;
    const unsigned tv0 = now();
    if (v == 0 && lane < F_N) ((LAS unsigned*)(L + L_FLAGS))[lane] = 0u;
    if (v == 7) {
      u64 pc; unsigned rel;
      asm volatile("s_getpc_b64 %0\nLpf_here:\n\ts_mov_b32 %1, Lpf_here-decode_block" : "=s"(pc), "=s"(rel));
      dma_df(mk((const void*)(pc - rel), CODEPF * 1024), lane * 16, r * 1024, L_OB + 7 * 2048);
    }
    wg_barrier();
    const rsrc_t rsV = mk(a.vc + kvoff, 131072);
    v4u vr[16];
    if (hf == 1) {
      const v4u z = {0u, 0u, 0u, 0u};
      if (j < 2) sth(rsW, WS_OP + (xc * 32 + r) * 2048 + (j * 64 + lane) * 16, z);
      if (r < 16) { if (j == 2) sth(rsW, WS_AP + (((4 * (r >> 3) + G) * 8 + xc) * 512 + 64 * (r & 7) + 4 * i16) * 4, z); }
      else if (isC) sth(rsW, WS_FP + (((cb0 + (G >> 1)) * 16 + 4 * j + (i16 >> 2)) * 28 + fg) * 128 + ((G & 1) * 4 + (i16 & 3)) * 16, z);
    }
    if (PFC && hf == 1) {
      if (T_PF) pause_until(tv0 + T_PF);
      const int fg0 = (14 * xc) >> 2;
      if (PFC & 1) {
        const rsrc_t rsG = mk(a.wg, 512 * FF * 2), rsU = mk(a.wu, 512 * FF * 2);
#pragma unroll
        for (int i = 0; i < 2; ++i) {
          dma_df(rsG, (lane >> 5) * (FF * 2) + (lane & 31) * 16, (16 * r + 4 * j + 2 * i) * (FF * 2) + 128 * fg0, L_OB + v * 2048);
          dma_df(rsU, (lane >> 5) * (FF * 2) + (lane & 31) * 16, (16 * r + 4 * j + 2 * i) * (FF * 2) + 128 * fg0, L_OB + v * 2048 + 1024);
        }
      }
      if (PFC & 2) {
        const rsrc_t rsD = mk(a.wd, FF * 512 * 2);
#pragma unroll
        for (int i = 0; i < 2; ++i) dma_df(rsD, lane * 16, (64 * fg0 + 8 * r + 2 * j + i) * 1024, L_OB + v * 2048 + i * 1024);
      }
      if (PFC & 4) {
        const rsrc_t rsO = mk(a.wo, 4096 * 512 * 2);
#pragma unroll
        for (int i = 0; i < 4; ++i) { dma_df(rsO, lane * 16, (512 * xc + 16 * r + 4 * j + i) * 1024, L_OB + v * 2048 + (i & 1) * 1024); }
      }
    }
    lwait(L, F_KI + j, 1 + hf);
#pragma unroll
    for (int i = 0; i < 16; ++i) { vr[i] = __builtin_amdgcn_raw_buffer_load_b128(rsV, lane * 16 + j * 1024, (hf * 16 + i) * 4096, KVPOL); if (hf) vmw<DEPTHV - 1>(); else vmw<DEPTH - 1>(); }
    __builtin_amdgcn_sched_barrier(0);
    lsignal(F_KI + j);
    lsignal(F_VI);
    __builtin_amdgcn_s_setprio(3);
    STAMP(1);
    unsigned s2[2][2];
#pragma unroll
    for (int hs = 0; hs < 2; ++hs)
#pragma unroll
      for (int d = 0; d < 2; ++d) {
        const unsigned b01 = hs ? 0x0302u : 0x0100u;
        s2[hs][d] = (d == ((lane >> 1) & 1)) ? ((lane & 1) ? ((b01 << 16) | 0x0c0cu) : (0x0c0c0000u | b01)) : 0x0c0c0c0cu;
      }
    lwait(L, F_L, 4);
    STAMP(2);
    const lptr pb = L + L_PB + (((j * 2 + hf) * 4 + G) * 4 + (i16 >> 2)) * 32;
    const v4u pw[2] = {*(LAS v4u*)pb, *(LAS v4u*)(pb + 16)};
    float fs;
    {
      const LAS float* ms = (const LAS float*)(L + L_MST) + G * 2;
      const float M = fmaxf(fmaxf(ms[0], ms[8]), fmaxf(ms[16], ms[24]));
      fs = __builtin_amdgcn_exp2f((ms[j * 8] - M) * LOG2E);
    }
    tag = __builtin_amdgcn_readfirstlane(lflag(L, F_TAG));
    f32x4 acc[2] = {{0.f, 0.f, 0.f, 0.f}, {0.f, 0.f, 0.f, 0.f}};
#pragma unroll
    for (int i = 0; i < 16; ++i) {
      const unsigned src = pw[i >> 3][(i >> 1) & 3];
      const v2u A = {__builtin_amdgcn_perm(src, src, s2[i & 1][0]), __builtin_amdgcn_perm(src, src, s2[i & 1][1])};
      acc[0] = mfma16(A, v2u{vr[i].x, vr[i].y}, acc[0]);
      acc[1] = mfma16(A, v2u{vr[i].z, vr[i].w}, acc[1]);
    }
    STAMP(3);
#pragma unroll
    for (int q = 0; q < 2; ++q) *(LAS f32x4*)(L + L_OB + v * 2048 + lane * 32 + q * 16) = acc[q] * fs;
    if (larrive(F_V) == 7u) {
      f32x4 p[2][8];
#pragma unroll
      for (int q = 0; q < 2; ++q)
#pragma unroll
        for (int k = 0; k < 8; ++k) p[q][k] = *(LAS f32x4*)(L + L_OB + k * 2048 + (q * 64 + lane) * 16);
#pragma unroll
      for (int q = 0; q < 2; ++q)
        sth(rsW, WS_OP + (xc * 32 + r) * 2048 + (q * 64 + lane) * 16, enc<8>(((p[q][0] + p[q][1]) + (p[q][2] + p[q][3])) + ((p[q][4] + p[q][5]) + (p[q][6] + p[q][7])), tag));
      lsignal(F_PUB);
      STAMP(4);
    }
    if (!isC || ABL >= 1) EXIT();
    unsigned sel[2][4];
    make_sel(sel, lane);
    if (hf != 0) {
      wg_barrier();
      v4u wf[2][8];
      c_load_w<2>(L, wf, 256 + 64 * j, lane);
      wg_barrier();
      wg_barrier();
      c_gate_up<2>(L, wf, sel, 256 + 64 * j, 8 + j, lane);
      wg_barrier();
      EXIT();
    }
    lwait(L, F_PUB, 1);
    v4u dw[16];
    {
      const rsrc_t rsD = mk(a.wd, FF * 512 * 2);
#pragma unroll
      for (int i = 0; i < 16; ++i) dw[i] = __builtin_amdgcn_raw_buffer_load_b128(rsD, G * 16384 + i16 * 16 + j * 256, (64 * fg + i) * 1024, WPOL);
      __builtin_amdgcn_sched_barrier(0);
    }
    wg_barrier();
    wg_barrier();
    wg_barrier();
    wg_barrier();
    f32x4 p[12];
#pragma unroll
    for (int k = 0; k < 12; ++k) p[k] = *(LAS f32x4*)(L + L_OB + k * 1024 + lane * 16);
    const f32x4 gu = (((p[0] + p[1]) + (p[2] + p[3])) + ((p[4] + p[5]) + (p[6] + p[7]))) + ((p[8] + p[9]) + (p[10] + p[11]));
    float av[4];
#pragma unroll
    for (int d = 0; d < 4; ++d) { const float t = rb(gu[d]); av[d] = rb(silu_f(t)) * dpp<ROR8>(t); }
    const lptr ab = L + L_RED + 2048 + j * 512;
    *(LAS v2u*)(ab + (G >> 1) * 256 + (8 * i16 + 4 * (G & 1)) * 2) = v2u{pk2(av[0], av[1]), pk2(av[2], av[3])};
    CBAR();
    const v4u a0 = *(LAS v4u*)(ab + ((lane >> 3) & 1) * 256 + G * 32), a1 = *(LAS v4u*)(ab + ((lane >> 3) & 1) * 256 + G * 32 + 16);
    f32x4 fa[2] = {{0.f, 0.f, 0.f, 0.f}, {0.f, 0.f, 0.f, 0.f}};
#pragma unroll
    for (int i = 0; i < 16; ++i) fa[i & 1] = mfma(diag(i < 8 ? a0[(i >> 1) & 3] : a1[(i >> 1) & 3], sel[i & 1]), dw[i], fa[i & 1]);
    sth(rsW, WS_FP + (((cb0 + (G >> 1)) * 16 + 4 * j + (i16 >> 2)) * 28 + fg) * 128 + ((G & 1) * 4 + (i16 & 3)) * 16, enc<8>(fa[0] + fa[1], tag));
    STAMP(10);
    EXIT();
  } else if (w >= 4) {
    const int j = w - 4;
    const rsrc_t rsK = mk(a.kc + kvoff, 131072);
    unsigned voff[4];
#pragma unroll
    for (int q = 0; q < 4; ++q) voff[q] = j * 1024 + G * 256 + ((i16 ^ (4 * q + G)) << 4);
#pragma unroll
    for (int i = 0; i < DEPTH - 1; ++i) DMA_K(rsK, voff[i & 3], i * 4096, i * 4096 + j * 1024);
    wg_barrier();
#pragma unroll
    for (int i = DEPTH - 1; i < 24; ++i) { DMA_K(rsK, voff[i & 3], i * 4096, i * 4096 + j * 1024); VMP(); }
    lwait(L, F_EW, 4);
#pragma unroll
    for (int i = 24; i < 32; ++i) { DMA_K(rsK, voff[i & 3], i * 4096, i * 4096 + j * 1024); VMP(); }
    lsignal(F_KI + j);
    STAMP(1);
    VM0();
    STAMP(2);
    const lptr kb = L + j * 1024 + (i16 >> 2) * 4096 + (i16 & 3) * 256;
    v4u kf[8][4];
#define KREAD(u) _Pragma("unroll") for (int ks = 0; ks < 4; ++ks) kf[u][ks] = *(LAS v4u*)(kb + (u) * 16384 + (((4 * ks + G) ^ i16) << 4))
    KREAD(0); KREAD(4); KREAD(1); KREAD(5); KREAD(2);
    __builtin_amdgcn_sched_barrier(0);
    lwait(L, F_QR, 4);
    STAMP(3);
    const int h = i16 & 3, rho = i16 >> 2;
    v4u qf[4];
#pragma unroll
    for (int ks = 0; ks < 4; ++ks) qf[ks] = *(LAS v4u*)(L + L_QS + h * 256 + ks * 64 + G * 16);
    tag = lflag(L, F_TAG);
    f32x4 sc[2] = {{0.f, 0.f, 0.f, 0.f}, {0.f, 0.f, 0.f, 0.f}};
#define KMMA(rp) _Pragma("unroll") for (int ks = 0; ks < 4; ++ks) { const v4u qm = (rho == (rp)) ? qf[ks] : v4u{0u, 0u, 0u, 0u}; \
      sc[0] = mfma(kf[rp][ks], qm, sc[0]); sc[1] = mfma(kf[4 + (rp)][ks], qm, sc[1]); }
    __builtin_amdgcn_sched_barrier(0);
    KMMA(0);
    __builtin_amdgcn_sched_barrier(0);
    KREAD(6); KREAD(3);
    __builtin_amdgcn_sched_barrier(0);
    KMMA(1);
    __builtin_amdgcn_sched_barrier(0);
    KREAD(7);
    __builtin_amdgcn_sched_barrier(0);
    KMMA(2); KMMA(3);
    tag = __builtin_amdgcn_readfirstlane(tag);
    float s[8];
#pragma unroll
    for (int k = 0; k < 8; ++k) s[k] = rb(rb(sc[k >> 2][k & 3]) * 0.08838834764831845f);
    float m = fmaxf(fmaxf(fmaxf(s[0], s[1]), fmaxf(s[2], s[3])), fmaxf(fmaxf(s[4], s[5]), fmaxf(s[6], s[7])));
    m = fmaxf(m, dpp<ROR4>(m)); m = fmaxf(m, dpp<ROR8>(m)); m = x32_max(x16_max(m));
    float l = 0.f;
#pragma unroll
    for (int k = 0; k < 8; ++k) {
      const float p = __builtin_amdgcn_exp2f((s[k] - m) * LOG2E);
      l += p;
      *(LAS __bf16*)(L + L_PB + ((((j * 2 + (k >> 2)) * 4 + (k & 3)) * 4 + h) * 16 + 4 * rho + G) * 2) = (__bf16)p;
    }
    l += dpp<ROR4>(l); l += dpp<ROR8>(l); l = x32_sum(x16_sum(l));
    if (lane < 4) { ((LAS float*)(L + L_MST))[(j * 4 + lane) * 2] = m; ((LAS float*)(L + L_MST))[(j * 4 + lane) * 2 + 1] = l; }
    if (larrive(F_L) == 3u) {
      if (lane < 4) {
        const LAS float* ms = (const LAS float*)(L + L_MST) + lane * 2;
        const float M = fmaxf(fmaxf(ms[0], ms[8]), fmaxf(ms[16], ms[24]));
        float Ls = 0.f;
#pragma unroll
        for (int k = 0; k < 4; ++k) Ls += ms[k * 8 + 1] * __builtin_amdgcn_exp2f((ms[k * 8] - M) * LOG2E);
        sth(rsW, WS_ST + (((xc * 8 + (r >> 2)) * 4 + lane) * 4 + (r & 3)) * 16, v4u{__float_as_uint(M), tag, __float_as_uint(Ls), ~tag});
      }
    }
    STAMP(4);
  } else {
    const int cw = w;
    const unsigned tstart = now();
    const bool isM = r < 24;
    const int ksq = r & 3, cgq = r >> 2;
    const int mg = cgq < 2 ? 2 * xc + cgq : 16 + 4 * xc + (cgq - 2);
    const rsrc_t rsM = mk(a.wm, 512 * 3072 * 2), rsQ = mk(a.wq, 512 * 4096 * 2), rsC = mk(a.c, 8192), rsX = mk(a.x, 8192);
    const int krow = 128 * ksq + 32 * cw;
    const unsigned ep = __builtin_amdgcn_raw_buffer_load_b32(rsW, WS_EPOCH, 0, SC1);
    const v4u cf = __builtin_amdgcn_raw_buffer_load_b128(rsC, (lane & 7) * 1024 + (krow + 8 * G) * 2, 0, 0);
    v4u xr[2];
#pragma unroll
    for (int i = 0; i < 2; ++i) xr[i] = __builtin_amdgcn_raw_buffer_load_b128(rsX, (2 * cw + i) * 1024 + lane * 16, 0, 0);
    const v4u xf = __builtin_amdgcn_raw_buffer_load_b128(rsX, (lane & 7) * 1024 + (krow + 8 * G) * 2, 0, 0);
    __builtin_amdgcn_sched_barrier(0);
    const bool crit = cgq < 2;
#define DMA_M() _Pragma("unroll") for (int i = 0; i < 4; ++i) dma_nt(rsM, (lane >> 3) * 6144 + (lane & 7) * 16, (krow + 8 * i) * 6144 + 128 * mg, L_EM + cw * 4096 + i * 1024)
#define DMA_Q() _Pragma("unroll") for (int i = 0; i < 4; ++i) dma_nt(rsQ, (lane >> 3) * 8192 + (lane & 7) * 16, (krow + 8 * i) * 8192 + (512 * xc + 64 * cgq) * 2, L_EQ + cw * 4096 + i * 1024)
    if (isM) DMA_M();
    DMA_Q();
    wg_barrier();
    tag = __builtin_amdgcn_readfirstlane(ep) + 1u;
    v4u sf;
#pragma unroll
    for (int d = 0; d < 4; ++d) sf[d] = pk2(silu_f(bflo(cf[d])), silu_f(bfhi(cf[d])));
    asm volatile("" : "+v"(sf));
    asm volatile("s_waitcnt vmcnt(4)" ::: "memory");
    STAMP(1);
    if (cw == 0 && lane == 0) ((LAS unsigned*)(L + L_FLAGS))[F_TAG] = tag;
    const int troff = cw * 4096 + (8 * G + (i16 >> 2)) * 128 + (i16 & 3) * 8;
    if (isM) {
      v4u wmf[4];
#pragma unroll
      for (int t = 0; t < 4; ++t) wmf[t] = trfrag(L + L_EM + troff + t * 32, 4 * 128);
      const int rd = crit ? (lane & 7) * 16 + (lane >> 3) : lane, rd2 = crit ? 8 : 64;
      const int st = crit ? WS_MC + (ksq * 16 + 2 * (mg & 7)) * 2048 + ((lane >> 3) & 1) * 1024 + (lane >> 4) * 256 + (mg >> 3) * 128 + (lane & 7) * 16
                          : WS_MP + ((ksq * 8 + G) * 2048 + 64 * (mg - 16) + 4 * i16) * 4;
      gemv_unit(L, rsW, wmf, sf, cw, lane, F_M, rd, rd2, st, crit ? 2048 : 4 * 2048 * 4, tag);
    }
    STAMP(2);
    VM0();
    v4u wqf[4];
#pragma unroll
    for (int t = 0; t < 4; ++t) wqf[t] = trfrag(L + L_EQ + troff + t * 32, 4 * 128);
    LG0();
    asm volatile("" : "+v"(wqf[0]), "+v"(wqf[1]), "+v"(wqf[2]), "+v"(wqf[3]));
    lsignal(F_EW);
#pragma unroll
    for (int i = 0; i < 2; ++i) {
      float e[8];
#pragma unroll
      for (int d = 0; d < 4; ++d) { e[2 * d] = bflo(xr[i][d]); e[2 * d + 1] = bfhi(xr[i][d]); }
      const float mean = wave_sum(((e[0] + e[1]) + (e[2] + e[3])) + ((e[4] + e[5]) + (e[6] + e[7]))) * (1.f / 512.f);
      float q = 0.f;
#pragma unroll
      for (int d = 0; d < 8; ++d) q += (e[d] - mean) * (e[d] - mean);
      const float var = wave_sum(q) * (1.f / 512.f);
      if (lane == 0) { ((LAS float*)(L + L_STAT))[(2 * cw + i) * 2] = rb(mean); ((LAS float*)(L + L_STAT))[(2 * cw + i) * 2 + 1] = rb(__builtin_amdgcn_sqrtf(rb(rb(var) + 1e-5f))); }
    }
    lsignal(F_LN);
    lwait(L, F_LN, 4);
    float nx[8];
    {
      const float mu = ((LAS float*)(L + L_STAT))[(lane & 7) * 2], rd = __builtin_amdgcn_rcpf(((LAS float*)(L + L_STAT))[(lane & 7) * 2 + 1]);
#pragma unroll
      for (int d = 0; d < 4; ++d) { nx[2 * d] = rb(rb(bflo(xf[d]) - mu) * rd); nx[2 * d + 1] = rb(rb(bfhi(xf[d]) - mu) * rd); }
    }
    {
      v4u g[8];
      const int o0 = WS_MC + (4 * ksq + cw) * 2048 + lane * 16;
      constexpr int PS = 16 * 2048;
      const int off[8] = {o0, o0 + 1024, o0 + PS, o0 + PS + 1024, o0 + 2 * PS, o0 + 2 * PS + 1024, o0 + 3 * PS, o0 + 3 * PS + 1024};
      pause_until(tstart + T_H1);
      if (ABL < 2) gather<4, 8, 16>(rsW, off, g, tag);
      STAMP(3);
      const f32x4 m0 = (asf(g[0]) + asf(g[2])) + (asf(g[4]) + asf(g[6])), m1 = (asf(g[1]) + asf(g[3])) + (asf(g[5]) + asf(g[7]));
      float hv[8];
#pragma unroll
      for (int d = 0; d < 8; ++d) {
        const float sa = rb(d < 4 ? m0[d & 3] : m1[d & 3]), ca = dpp<ROR8>(sa);
        hv[d] = rb(nx[d] * rb(1.f + ca)) + sa;
      }
      const v4u hf = {pk2(hv[0], hv[1]), pk2(hv[2], hv[3]), pk2(hv[4], hv[5]), pk2(hv[6], hv[7])};
      gemv_unit(L, rsW, wqf, hf, cw, lane, F_Q, lane, 64, WS_QP + ((ksq * 8 + G) * 4096 + 512 * xc + 64 * cgq + 4 * i16) * 4, 4 * 4096 * 4, tag);
    }
    STAMP(4);
    {
      v4u g[2];
      const int o0 = WS_QP + (((lane >> 5) * 8 + (r >> 2)) * 4096 + 512 * xc + 128 * cw + 4 * (lane & 31)) * 4;
      const int off[2] = {o0, o0 + 2 * 8 * 4096 * 4};
      pause_until(now() + T_H2);
      if (ABL < 2) gather<4, 2, 16>(rsW, off, g, tag);
      const f32x4 s = asf(g[0]) + asf(g[1]);
      const float q0 = x32_sum(s.x), q1 = x32_sum(s.y), q2 = x32_sum(s.z), q3 = x32_sum(s.w);
      if (lane < 32) *(LAS v2u*)(L + L_QS + cw * 256 + lane * 8) = v2u{pk2(q0, q1), pk2(q2, q3)};
      lsignal(F_QR);
    }
    STAMP(5);
    if (cw == 0 && ABL < 2) {
      v4u g[1];
      const int off[1] = {WS_MP + ((G * 8 + xc) * 2048 + 64 * r + 4 * i16) * 4};
      gather<4, 1, 8>(rsW, off, g, tag);
      const f32x4 s = asf(g[0]);
      f32x4 o;
#pragma unroll
      for (int d = 0; d < 4; ++d) o[d] = rb(x32_sum(x16_sum(s[d])));
      if (lane < 16) sth(rsW, WS_MOD + (xc * 2048 + 64 * r + 4 * i16) * 4, __builtin_bit_cast(v4u, o) | tagv<16>(tag));
    }
  }

  if (r >= 30 || ABL >= 1) EXIT();
  const int tw = w;
  lwait(L, F_L, 4);
  lwait(L, F_VI, 8);
  STAMP(6);
  if (r < 16) {
    const int cg = r & 7, bg = r >> 3;
    {
      const rsrc_t rsO = mk(a.wo, 4096 * 512 * 2);
#pragma unroll
      for (int i = 0; i < 8; ++i) DMA_W(rsO, (lane >> 3) * 1024 + (lane & 7) * 16, (512 * xc + 64 * tw + 8 * i) * 1024 + 128 * cg, tw * 8192 + i * 1024), vmw<DEPTHW - 1>();
    }
    STAMP(14);
    float scl[4];
    {
      v4u g;
#pragma unroll 1
      for (unsigned i = 0; i < GSPIN; ++i) {
        CBAR();
        g = ldh(rsW, WS_ST + (((xc * 8 + 4 * bg + G) * 4 + (tw >> 1)) * 4 + (lane & 3)) * 16);
        if (__all(g.y == tag && g.w == ~tag)) break;
        __builtin_amdgcn_s_sleep(2);
      }
      const float ms = __uint_as_float(g.x), ls = __uint_as_float(g.z);
      float M = fmaxf(ms, dpp<XOR1>(ms)); M = fmaxf(M, dpp<XOR2>(M));
      const float e = __builtin_amdgcn_exp2f((ms - M) * LOG2E);
      float den = e * ls; den += dpp<XOR1>(den); den += dpp<XOR2>(den);
      const float sc = e * __builtin_amdgcn_rcpf(den);
      scl[0] = dpp<0x00>(sc); scl[1] = dpp<0x55>(sc); scl[2] = dpp<0xAA>(sc); scl[3] = dpp<0xFF>(sc);
    }
    STAMP(15);
    VM0();
    v4u wf[2][4];
#pragma unroll
    for (int kk = 0; kk < 2; ++kk)
#pragma unroll
      for (int t = 0; t < 4; ++t) wf[kk][t] = trfrag(L + tw * 8192 + (32 * kk + 8 * G + (i16 >> 2)) * 128 + t * 32 + (i16 & 3) * 8, 4 * 128);
    STAMP(7);
    lwait(L, F_PUB, 1);
    STAMP(8);
    {
      v4u g[4];
      const int o0 = WS_OP + (xc * 32 + (4 * bg + G) * 4) * 2048 + tw * 256 + i16 * 16;
      const int off[4] = {o0, o0 + 2048, o0 + 4096, o0 + 6144};
      gather<8, 4, 0>(rsW, off, g, tag);
      STAMP(9);
      const f32x4 o = (asf(g[0]) * scl[0] + asf(g[1]) * scl[1]) + (asf(g[2]) * scl[2] + asf(g[3]) * scl[3]);
      const lptr ob = L + 65536 + tw * 512;
      *(LAS v2u*)(ob + lane * 8) = v2u{pk2(o.x, o.y), pk2(o.z, o.w)};
      CBAR();
      const v4u of0 = *(LAS v4u*)(ob + (lane & 3) * 128 + G * 16), of1 = *(LAS v4u*)(ob + (lane & 3) * 128 + 64 + G * 16);
      f32x4 acc[4];
#pragma unroll
      for (int t = 0; t < 4; ++t) acc[t] = mfma(wf[0][t], of0, f32x4{0.f, 0.f, 0.f, 0.f});
#pragma unroll
      for (int t = 0; t < 4; ++t) acc[t] = mfma(wf[1][t], of1, acc[t]);
      const int rep = i16 >> 2;
      const f32x4 d = rep == 0 ? acc[0] : rep == 1 ? acc[1] : rep == 2 ? acc[2] : acc[3];
      *(LAS f32x4*)(L + L_RED + tw * 1024 + (lane & 3) * 256 + rep * 64 + G * 16) = d;
    }
    wg_barrier();
    if (tw == 0) {
      f32x4 p[8];
#pragma unroll
      for (int k = 0; k < 8; ++k) p[k] = *(LAS f32x4*)(L + L_RED + k * 1024 + lane * 16);
      sth(rsW, WS_AP + (((4 * bg + G) * 8 + xc) * 512 + 64 * cg + 4 * i16) * 4, enc<8>(((p[0] + p[1]) + (p[2] + p[3])) + ((p[4] + p[5]) + (p[6] + p[7])), tag));
      STAMP(10);
    }
    if (tw != 1) EXIT();
    const int col = 32 * r + 8 * (lane & 3) + (lane & 4), pg = lane >> 3;
    const v2u xx = __builtin_amdgcn_raw_buffer_load_b64(mk(a.x, 8192), (xc * 512 + col) * 2, 0, 0);
    v4u md[2];
    {
      const int off[2] = {WS_MOD + (xc * 2048 + col) * 4, WS_MOD + (xc * 2048 + 1536 + col) * 4};
      gather<16, 2, 2>(rsW, off, md, tag);
    }
    f32x4 x2;
    {
      v4u p[1];
      const int off1[1] = {WS_AP + ((xc * 8 + pg) * 512 + col) * 4};
      gather<8, 1, 2>(rsW, off1, p, tag);
      f32x4 s = asf(p[0]);
#pragma unroll
      for (int d = 0; d < 4; ++d) { float t = s[d]; t += dpp<ROR8>(t); s[d] = x32_sum(x16_sum(t)); }
      const f32x4 ga = asf(md[0]);
      x2.x = rb(bflo(xx.x) + rb(ga.x * rb(s.x))); x2.y = rb(bfhi(xx.x) + rb(ga.y * rb(s.y)));
      x2.z = rb(bflo(xx.y) + rb(ga.z * rb(s.z))); x2.w = rb(bfhi(xx.y) + rb(ga.w * rb(s.w)));
    }
    STAMP(11);
    {
      v4u g[4];
      const int o0 = WS_FP + (xc * 16 + r) * 3584 + lane * 16;
      const int off[4] = {o0, o0 + 1024, o0 + 2048, lane < 32 ? o0 + 3072 : o0};
      gather<8, 4, 0>(rsW, off, g, tag);
      STAMP(12);
      f32x4 s = (asf(g[0]) + asf(g[1])) + asf(g[2]);
      if (lane < 32) s += asf(g[3]);
#pragma unroll
      for (int d = 0; d < 4; ++d) { float t = s[d]; t += dpp<ROR8>(t); s[d] = x32_sum(x16_sum(t)); }
      const f32x4 gb = asf(md[1]);
      const float o0f = x2.x + rb(gb.x * rb(s.x)), o1 = x2.y + rb(gb.y * rb(s.y)), o2 = x2.z + rb(gb.z * rb(s.z)), o3 = x2.w + rb(gb.w * rb(s.w));
      if (lane < 8) __builtin_amdgcn_raw_buffer_store_b64(v2u{pk2(o0f, o1), pk2(o2, o3)}, mk(a.out, 8192), (xc * 512 + col) * 2, 0, 0);
    }
    STAMP(13);
  } else {
    const int b = cb0 + (tw >> 2), col = 128 * (tw & 3) + 4 * (lane & 31);
    {
      const rsrc_t rsG = mk(a.wg, 512 * FF * 2), rsU = mk(a.wu, 512 * FF * 2);
      const int rot = 0;
#pragma unroll
      for (int i = 0; i < 8; ++i) { const int p = (i + rot) & 7;
        DMA_W(rsG, (lane >> 3) * (FF * 2) + (lane & 7) * 16, (64 * tw + 8 * p) * (FF * 2) + 128 * fg, tw * 8192 + p * 1024); vmw<DEPTHW - 1>();
        DMA_W(rsU, (lane >> 3) * (FF * 2) + (lane & 7) * 16, (64 * tw + 8 * p) * (FF * 2) + 128 * fg, 65536 + tw * 8192 + p * 1024); vmw<DEPTHW - 1>(); }
    }
    STAMP(11);
    unsigned sel[2][4];
    make_sel(sel, lane);
    const v2u xx = __builtin_amdgcn_raw_buffer_load_b64(mk(a.x, 8192), (b * 512 + col) * 2, 0, 0);
    v4u md[3];
    {
      const int o0 = WS_MOD + (b * 2048 + col) * 4;
      const int off[3] = {o0, o0 + 512 * 4, o0 + 1024 * 4};
      gather<16, 3, 2>(rsW, off, md, tag);
    }
    VM0();
    STAMP(12);
    wg_barrier();
    v4u wf[1][8];
    c_load_w<1>(L, wf, 32 * tw, lane);
    STAMP(7);
    const f32x4 ga = asf(md[0]), sb = asf(md[1]);
    f32x4 cb1 = asf(md[2]);
#pragma unroll
    for (int d = 0; d < 4; ++d) cb1[d] = rb(1.f + cb1[d]);
    float x2[4];
    {
      v4u g[4];
      const int o0 = WS_AP + ((b * 8 + 4 * (lane >> 5)) * 512 + col) * 4;
      const int off[4] = {o0, o0 + 2048, o0 + 4096, o0 + 6144};
      gather<8, 4, 0>(rsW, off, g, tag);
      STAMP(9);
      const f32x4 s = (asf(g[0]) + asf(g[1])) + (asf(g[2]) + asf(g[3]));
      x2[0] = rb(bflo(xx.x) + rb(ga.x * rb(x32_sum(s.x)))); x2[1] = rb(bfhi(xx.x) + rb(ga.y * rb(x32_sum(s.y))));
      x2[2] = rb(bflo(xx.y) + rb(ga.z * rb(x32_sum(s.z)))); x2[3] = rb(bfhi(xx.y) + rb(ga.w * rb(x32_sum(s.w))));
    }
    if (wg == 128 && tw == 0 && lane == 0) __builtin_amdgcn_raw_buffer_store_b32(tag, rsW, WS_EPOCH, 0, SC1);
    const float c0 = __builtin_bit_cast(float, __builtin_amdgcn_readfirstlane(__builtin_bit_cast(int, x2[0])));
    const float d0 = x2[0] - c0, d1 = x2[1] - c0, d2 = x2[2] - c0, d3 = x2[3] - c0;
    const float s1 = half_sum((d0 + d1) + (d2 + d3)), s2 = half_sum((d0 * d0 + d1 * d1) + (d2 * d2 + d3 * d3));
    if (lane == 0) { ((LAS float*)(L + L_STAT))[tw * 2] = c0 + s1 * (1.f / 128.f); ((LAS float*)(L + L_STAT))[tw * 2 + 1] = s2 - s1 * s1 * (1.f / 128.f); }
    wg_barrier();
    const f32x4 sa = *(LAS f32x4*)(L + L_STAT + (tw >> 2) * 32), sc = *(LAS f32x4*)(L + L_STAT + (tw >> 2) * 32 + 16);
    const float mean = 0.25f * ((sa.x + sa.z) + (sc.x + sc.z));
    const float e0 = sa.x - mean, e1 = sa.z - mean, e2 = sc.x - mean, e3 = sc.z - mean;
    const float var = fmaxf((((sa.y + sa.w) + (sc.y + sc.w)) + 128.f * ((e0 * e0 + e1 * e1) + (e2 * e2 + e3 * e3))) * (1.f / 512.f), 0.f);
    const float mu = rb(mean), rd = __builtin_amdgcn_rcpf(rb(__builtin_amdgcn_sqrtf(rb(rb(var) + 1e-5f))));
    float h2[4];
#pragma unroll
    for (int d = 0; d < 4; ++d) h2[d] = rb(rb(rb(x2[d] - mu) * rd) * cb1[d]) + sb[d];
    if (lane < 32) *(LAS v2u*)(L + L_RED + (tw >> 2) * 1024 + col * 2) = v2u{pk2(h2[0], h2[1]), pk2(h2[2], h2[3])};
    wg_barrier();
    c_gate_up<1>(L, wf, sel, 32 * tw, tw, lane);
    wg_barrier();
    STAMP(8);
  }
  EXIT();
}

extern "C" void kernel_launch(void* const* d_in, const int* in_sizes, int n_in, void* d_out, int out_size, void* d_ws, size_t ws_size, hipStream_t stream) {
  Args a;
  a.x = (const unsigned short*)d_in[0]; a.kc = (const unsigned short*)d_in[1]; a.vc = (const unsigned short*)d_in[2];
  a.wq = (const unsigned short*)d_in[3]; a.wo = (const unsigned short*)d_in[4];
  a.wg = (const unsigned short*)d_in[6]; a.wu = (const unsigned short*)d_in[7]; a.wd = (const unsigned short*)d_in[8];
  a.c = (const unsigned short*)d_in[9]; a.wm = (const unsigned short*)d_in[10];
  a.out = (unsigned short*)d_out; a.ws = (unsigned char*)d_ws;
  (void)hipFuncSetAttribute((const void*)decode_block, hipFuncAttributeMaxDynamicSharedMemorySize, L_TOTAL);
  hipLaunchKernelGGL(dummy_k, dim3(1), dim3(64), 0, stream, (unsigned*)((char*)d_ws + (8 << 20)));
  hipLaunchKernelGGL(decode_block, dim3(256), dim3(1024), L_TOTAL, stream, a);
}
```

```cpp
#include <hip/hip_runtime.h>
#include <hip/hip_bf16.h>
#include <stdint.h>

#define STAMPS 0
#define DEPTH 5
#define DEPTHV 10
#define DEPTHW 16
#define T_H1 480
#define T_H2 780
#define CODEPF 22
#define PREW2 0
#define EROT 0
#define DEPTHE 8
#define T_WB 0
#define T_WC 0
#define T_RB 0
#define T_RC 0
#define PREW 0
#define PRER 9
#define T_Q 0
#define T_M2 T_Q
#define CRITFIRST 1
#define ADAPT 232
#define ADAPT0 150
#define DEPTHA 10
#define PFE 0
#define PULL2 0
#define DEPTHU 8
#define LPFIRST 1
#define LPB 0
#define T_ST 1180
#define SELPOLL 0
#define PSLEEP 0
#define LPC 1
#define CWAIT 1
#define DNLAST 1
#define LPD 1
#define PF3 1
#define T_BP 30
#define T_K 0
#define PFH 0
#define T_PULL 830
#define DEPTHP 2
#define DEPTHM 8
#define INIT 5
#define RATE 0
#define RATE0 0
#define ABL 0
#define DRY 0
#define PFO 2

namespace {

typedef unsigned v4u __attribute__((ext_vector_type(4)));
typedef unsigned v2u __attribute__((ext_vector_type(2)));
typedef float f32x4 __attribute__((ext_vector_type(4)));
typedef float f32x2 __attribute__((ext_vector_type(2)));
typedef short s16x4 __attribute__((ext_vector_type(4)));
typedef __bf16 bf16x8 __attribute__((ext_vector_type(8)));
typedef __bf16 bf16x2 __attribute__((ext_vector_type(2)));
typedef unsigned long long u64;
typedef __amdgpu_buffer_rsrc_t rsrc_t;
#define LAS __attribute__((address_space(3)))
typedef LAS unsigned char* lptr;

constexpr int SK = 8192, WIN = 2048, FF = 1792;
constexpr int SC1 = 16, NT = 2;
constexpr int WS_EPOCH = 0;
constexpr int WS_MC = 4096;
constexpr int WS_MOD = WS_MC + 65536;
constexpr int WS_QP = WS_MOD + 131072;
constexpr int WS_OP = WS_QP + 262144;
constexpr int WS_ST = WS_OP + 524288;
constexpr int WS_AP = WS_ST + 16384;
constexpr int WS_FP = WS_AP + 131072;
constexpr int WS_TOTAL = WS_FP + 458752;
constexpr int WS_STAMP = 4 << 20;
constexpr int L_EW = 98304;
constexpr int L_FLAGS = 131072, L_QS = L_FLAGS + 256, L_MST = L_QS + 1024, L_STAT = L_MST + 128, L_PB = L_STAT + 128, L_OB = L_PB + 4096, L_RED = L_OB + 16384,
              L_TOTAL = L_RED + 8192;
constexpr int L_GU = L_RED + 4096;
static_assert(L_TOTAL <= 163840, "LDS");
enum { F_EW = 0, F_M = 1, F_LN = 2, F_ED = 3, F_QR = 4, F_L = 5, F_VI = 6, F_V = 7, F_PUB = 8, F_TAG = 9, F_KR = 10, F_TR = 11, F_KI = 12, F_V1 = 16, F_W = 20, F_UP = 21, F_N = 24 };
constexpr unsigned GSPIN = 1u << 16, LSPIN = 1u << 20;
constexpr float LOG2E = 1.4426950408889634f;

__device__ __forceinline__ float bflo(unsigned u) { return __uint_as_float(u << 16); }
__device__ __forceinline__ float bfhi(unsigned u) { return __uint_as_float(u & 0xffff0000u); }
__device__ __forceinline__ f32x2 unpk(unsigned u) { return f32x2{bflo(u), bfhi(u)}; }
__device__ __forceinline__ unsigned pk2(float a, float b) { bf16x2 p = {(__bf16)a, (__bf16)b}; unsigned u = __builtin_bit_cast(unsigned, p); asm("" : "+v"(u)); return u; }
__device__ __forceinline__ unsigned pk2(f32x2 v) { return pk2(v.x, v.y); }
__device__ __forceinline__ f32x2 rb2(f32x2 v) { return unpk(pk2(v)); }
__device__ __forceinline__ float rb(float a) { return bfhi(pk2(a, a)); }
__device__ __forceinline__ f32x2 lo2(f32x4 v) { return f32x2{v.x, v.y}; }
__device__ __forceinline__ f32x2 hi2(f32x4 v) { return f32x2{v.z, v.w}; }
template <int CTRL> __device__ __forceinline__ float dpp(float x) { return __builtin_bit_cast(float, __builtin_amdgcn_mov_dpp(__builtin_bit_cast(int, x), CTRL, 0xf, 0xf, true)); }
template <int CTRL> __device__ __forceinline__ unsigned dppu(unsigned x) { return (unsigned)__builtin_amdgcn_mov_dpp((int)x, CTRL, 0xf, 0xf, true); }
constexpr int XOR1 = 0xB1, XOR2 = 0x4E, HMIR = 0x141, ROR8 = 0x128, ROR4 = 0x124;
__device__ __forceinline__ float sw32(float a, float b) { auto s = __builtin_amdgcn_permlane32_swap(__float_as_uint(a), __float_as_uint(b), false, false); return __uint_as_float(s[0]) + __uint_as_float(s[1]); }
__device__ __forceinline__ float sw16(float a, float b) { auto s = __builtin_amdgcn_permlane16_swap(__float_as_uint(a), __float_as_uint(b), false, false); return __uint_as_float(s[0]) + __uint_as_float(s[1]); }
__device__ __forceinline__ float x16_sum(float x) { return sw16(x, x); }
__device__ __forceinline__ float x32_sum(float x) { return sw32(x, x); }
__device__ __forceinline__ float x16_max(float x) { auto s = __builtin_amdgcn_permlane16_swap(__float_as_uint(x), __float_as_uint(x), false, false); return fmaxf(__uint_as_float(s[0]), __uint_as_float(s[1])); }
__device__ __forceinline__ float x32_max(float x) { auto s = __builtin_amdgcn_permlane32_swap(__float_as_uint(x), __float_as_uint(x), false, false); return fmaxf(__uint_as_float(s[0]), __uint_as_float(s[1])); }
__device__ __forceinline__ float row_sum(float x) { x += dpp<XOR1>(x); x += dpp<XOR2>(x); x += dpp<HMIR>(x); x += dpp<ROR8>(x); return x; }
__device__ __forceinline__ float wave_sum(float x) { return x32_sum(x16_sum(row_sum(x))); }
__device__ __forceinline__ f32x4 mfma(v4u a, v4u b, f32x4 c) { return __builtin_amdgcn_mfma_f32_16x16x32_bf16(__builtin_bit_cast(bf16x8, a), __builtin_bit_cast(bf16x8, b), c, 0, 0, 0); }
__device__ __forceinline__ f32x4 mfma16(v2u a, v2u b, f32x4 c) { return __builtin_amdgcn_mfma_f32_16x16x16bf16_1k(__builtin_bit_cast(s16x4, a), __builtin_bit_cast(s16x4, b), c, 0, 0, 0); }
__device__ __forceinline__ float silu_f(float v) { return v * __builtin_amdgcn_rcpf(1.f + __builtin_amdgcn_exp2f(-v * LOG2E)); }
__device__ __forceinline__ rsrc_t mk(const void* p, int bytes) { return __builtin_amdgcn_make_buffer_rsrc((void*)p, 0, bytes, 0x00020000); }
template <class T> __device__ __forceinline__ T sel(bool c, T a, T b) { return c ? a : b; }
constexpr f32x4 Z4 = {0.f, 0.f, 0.f, 0.f};
__device__ __forceinline__ void dma_nt(rsrc_t rs, unsigned voff, unsigned soff, unsigned ldsaddr) {
  asm volatile("s_mov_b32 m0, %0\n\ts_nop 3\n\tbuffer_load_dwordx4 %1, %2, %3 offen nt lds" ::"s"(ldsaddr), "v"(voff), "s"(rs), "s"(soff) : "memory");
}
__device__ __forceinline__ void dma_df(rsrc_t rs, unsigned voff, unsigned soff, unsigned ldsaddr) {
  asm volatile("s_mov_b32 m0, %0\n\ts_nop 3\n\tbuffer_load_dwordx4 %1, %2, %3 offen lds" ::"s"(ldsaddr), "v"(voff), "s"(rs), "s"(soff) : "memory");
}
template <int N> __device__ __forceinline__ void vmw() { asm volatile("s_waitcnt vmcnt(%0)" ::"n"(N) : "memory"); }
#define LG0() asm volatile("s_waitcnt lgkmcnt(0)" ::: "memory")
#define CBAR() asm volatile("" ::: "memory")
#define SBAR() __builtin_amdgcn_sched_barrier(0)
__device__ __forceinline__ void wg_barrier() { LG0(); __builtin_amdgcn_s_barrier(); CBAR(); }
__device__ __forceinline__ v4u ldh(rsrc_t rs, int off) { return __builtin_amdgcn_raw_buffer_load_b128(rs, off, 0, SC1); }
__device__ __forceinline__ void sth(rsrc_t rs, int off, v4u v) { __builtin_amdgcn_raw_buffer_store_b128(v, rs, off, 0, SC1); }
__device__ __forceinline__ unsigned lflag(lptr L, int f) { return __hip_atomic_load((LAS unsigned*)(L + L_FLAGS) + f, __ATOMIC_RELAXED, __HIP_MEMORY_SCOPE_WORKGROUP); }
__device__ __forceinline__ void lwait(lptr L, int f, unsigned want) {
#pragma unroll 1
  for (unsigned i = 0; i < LSPIN; ++i) { if ((unsigned)__builtin_amdgcn_readfirstlane(lflag(L, f)) >= want) break; __builtin_amdgcn_s_sleep(1); }
  CBAR();
}
__device__ __forceinline__ unsigned larrive(int f) {
  unsigned old;
  asm volatile("s_mov_b64 exec, 1\n\tds_add_rtn_u32 %0, %1, %2\n\ts_mov_b64 exec, -1\n\ts_waitcnt lgkmcnt(0)" : "=&v"(old) : "v"(L_FLAGS + 4 * f), "v"(1u) : "memory");
  return __builtin_amdgcn_readfirstlane(old);
}
__device__ __forceinline__ void lsignal(int f) {
  asm volatile("s_mov_b64 exec, 1\n\tds_add_u32 %0, %1\n\ts_mov_b64 exec, -1" ::"v"(L_FLAGS + 4 * f), "v"(1u) : "memory");
}
__device__ __forceinline__ v4u trfrag(lptr p) {
  s16x4 a = __builtin_amdgcn_ds_read_tr16_b64_v4i16((LAS s16x4*)p), b = __builtin_amdgcn_ds_read_tr16_b64_v4i16((LAS s16x4*)(p + 512));
  v2u x = __builtin_bit_cast(v2u, a), y = __builtin_bit_cast(v2u, b);
  return v4u{x.x, x.y, y.x, y.y};
}
template <int BITS> __device__ __forceinline__ v4u tagv(unsigned tag) { constexpr unsigned m = (1u << BITS) - 1u; return v4u{tag & m, (tag + 1u) & m, (tag + 2u) & m, (tag + 3u) & m}; }
template <int BITS> __device__ __forceinline__ v4u enc(f32x4 v, unsigned tag) {
  constexpr unsigned m = (1u << BITS) - 1u;
  return ((__builtin_bit_cast(v4u, v) + (1u << (BITS - 1))) & ~m) | tagv<BITS>(tag);
}
template <int BITS, int N, int SLEEP, int STRIDE> __device__ __forceinline__ void gather(rsrc_t rs, int vo, int vlast, v4u (&v)[N], unsigned tag) {
  const v4u tv = tagv<BITS>(tag);
#pragma unroll 1
  for (unsigned i = 0; i < GSPIN; ++i) {
    CBAR();
#pragma unroll
    for (int k = 0; k < N; ++k) v[k] = __builtin_amdgcn_raw_buffer_load_b128(rs, k == N - 1 ? vlast : vo, k * STRIDE, SC1);
    unsigned bad = 0;
#pragma unroll
    for (int k = 0; k < N; ++k) { v[k] ^= tv; bad |= v[k].x | v[k].y | v[k].z | v[k].w; }
    if (__all((bad & ((1u << BITS) - 1u)) == 0u)) break;
    if (SLEEP) __builtin_amdgcn_s_sleep(SLEEP);
  }
}
template <int BITS, int N, int SLEEP, int STRIDE> __device__ __forceinline__ void gather(rsrc_t rs, int vo, v4u (&v)[N], unsigned tag) { gather<BITS, N, SLEEP, STRIDE>(rs, vo, vo, v, tag); }
__device__ __forceinline__ f32x4 asf(v4u v) { return __builtin_bit_cast(f32x4, v); }
__device__ __forceinline__ unsigned now() { return (unsigned)__builtin_amdgcn_s_memrealtime(); }
__device__ __forceinline__ void pause_until(unsigned t) {
#pragma unroll 1
  for (int i = 0; i < 4096 && (int)(now() - t) < 0; ++i) __builtin_amdgcn_s_sleep(2);
}
#define PACE(n) do { if (RATE) { const unsigned t_ = tw0 + RATE0 + (unsigned)(n) * RATE / 16u; _Pragma("unroll 1") for (int i_ = 0; i_ < 2048 && (int)(now() - t_) < 0; ++i_) __builtin_amdgcn_s_sleep(1); } } while (0)
#define AWAIT(n, D) do { if (ADAPT) { vmw<DEPTHA - 1>(); if ((int)(now() - (tw0 + ADAPT0 + (unsigned)(n) * ADAPT / 16u)) < 0) vmw<(D) - 1>(); } else vmw<(D) - 1>(); } while (0)
__device__ __forceinline__ void gemv_unit(lptr L, rsrc_t rsW, const v4u (&wf)[2][4], const v4u (&af)[2], int cw, int lane, int rd, int rd2, int st, int st2, bool late, unsigned tlate, unsigned tag) {
  const int G = lane >> 4, i16 = lane & 15;
  f32x4 acc[4];
#pragma unroll
  for (int t = 0; t < 4; ++t) acc[t] = mfma(wf[0][t], af[0], Z4);
#pragma unroll
  for (int t = 0; t < 4; ++t) acc[t] = mfma(wf[1][t], af[1], acc[t]);
  if (i16 < 8) {
#pragma unroll
    for (int t = 0; t < 4; ++t) *(LAS f32x4*)(L + L_RED + ((cw * 8 + i16) * 64 + 16 * t + 4 * G) * 4) = acc[t];
  }
  const unsigned old = larrive(F_M);
  if (late ? cw == 3 : old == 3u) {
    if (late) { lwait(L, F_M, 4); if (T_WB) pause_until(tlate); else lwait(L, F_VI, 4); }
    f32x4 p[2][4];
#pragma unroll
    for (int q = 0; q < 2; ++q)
#pragma unroll
      for (int k = 0; k < 4; ++k) p[q][k] = *(LAS f32x4*)(L + L_RED + k * 2048 + (rd + q * rd2) * 16);
    if (!(ABL & 32)) {
#pragma unroll
      for (int q = 0; q < 2; ++q) sth(rsW, st + q * st2, enc<4>((p[q][0] + p[q][1]) + (p[q][2] + p[q][3]), tag));
    }
  }
}
__device__ __forceinline__ void gu_load(lptr L, v4u (&wf)[2][4], int w, int lane) {
  const lptr p = L + (128 * (w >> 2) + 8 * (lane >> 4) + ((lane & 15) >> 2)) * 128 + (((w & 3) ^ ((lane >> 3) & 3)) * 32) + (lane & 3) * 8;
#pragma unroll
  for (int m = 0; m < 2; ++m)
#pragma unroll
    for (int ks = 0; ks < 4; ++ks) wf[m][ks] = trfrag(p + m * 65536 + ks * 4096);
}
__device__ __forceinline__ void gu_mma(lptr L, const v4u (&wf)[2][4], int w, int lane) {
  v4u hf[4];
#pragma unroll
  for (int ks = 0; ks < 4; ++ks) hf[ks] = *(LAS v4u*)(L + L_RED + (lane & 1) * 1024 + (128 * (w >> 2) + 32 * ks + 8 * (lane >> 4)) * 2);
  f32x4 acc[2] = {Z4, Z4};
#pragma unroll
  for (int ks = 0; ks < 4; ++ks) { acc[0] = mfma(wf[0][ks], hf[ks], acc[0]); acc[1] = mfma(wf[1][ks], hf[ks], acc[1]); }
  const f32x4 d = sel(lane & 2, acc[1], acc[0]);
  if ((lane & 12) == 0) *(LAS f32x4*)(L + L_GU + (w >> 2) * 1024 + (lane & 3) * 256 + (16 * (w & 3) + 4 * (lane >> 4)) * 4) = d;
}

struct Args {
  const unsigned short *x, *kc, *vc, *wq, *wo, *wg, *wu, *wd, *c, *wm;
  unsigned short* out;
  unsigned char* ws;
};

#define STAMP(i) do {} while (0)
#define FINE 0
#define FSTAMP(f, i) do {} while (0)
#define EXIT() return

}

extern "C" __global__ void __launch_bounds__(1024, 4) decode_block(Args a) {
  extern __shared__ __attribute__((aligned(16))) unsigned char smem[];
  const lptr L = (lptr)smem;
  const int lane = threadIdx.x & 63, w = __builtin_amdgcn_readfirstlane(threadIdx.x >> 6);
  const int wg = blockIdx.x, xc = wg & 7, r = wg >> 3;
  if (w == 15) {
    u64 pc; unsigned rel;
    asm volatile("s_getpc_b64 %0\nLpf_here:\n\ts_mov_b32 %1, Lpf_here-decode_block" : "=s"(pc), "=s"(rel));
    dma_df(mk((const void*)(pc - rel), CODEPF * 1024), lane * 16, r * 1024, L_OB + 7 * 2048);
  }
  const int G = lane >> 4, i16 = lane & 15;
  const size_t kvoff = ((size_t)((r >> 2) * 8 + xc) * SK + (SK - WIN) + 512 * (r & 3)) * 128;
  const rsrc_t rsW = mk(a.ws, WS_TOTAL);
  const bool isC = r >= 16 && r < 30;
  const int cidx = 14 * xc + (r - 16), fg = cidx >> 2, cb0 = 2 * (cidx & 3);
  const int er = r ^ EROT;
  const bool early = er < 28;
  STAMP(0);
  unsigned tag;
  const unsigned tw0 = now();

  if (w >= 8) {
    const int v = w - 8, j = v & 3, hf = v >> 2;
    if (v == 0 && lane < F_N) ((LAS unsigned*)(L + L_FLAGS))[lane] = 0u;
    wg_barrier();
    const rsrc_t rsV = mk(a.vc + kvoff, 131072);
    v4u vr[16];
    if (PREW && hf == 1 && !(ABL & 16)) {
      const v4u z = {0u, 0u, 0u, 0u};
      if (j < 2) sth(rsW, WS_OP + (xc * 32 + r) * 2048 + (j * 64 + lane) * 16, z);
      if (r < 16) { if (j == 2) sth(rsW, WS_AP + (((4 * (r >> 3) + G) * 8 + xc) * 512 + 64 * (r & 7) + 4 * i16) * 4, z); }
      else if (isC) sth(rsW, WS_FP + (((cb0 + (j >> 1)) * 16 + 8 * (j & 1) + (lane >> 3)) * 28 + fg) * 128 + (lane & 7) * 16, z);
    }
    lwait(L, F_KI + j, 1 + hf);
    constexpr int NPF = ((PFE & 1) ? 4 : 0) + ((PFE & 2) ? 2 : 0) + ((PFE & 4) ? 2 : 0) + ((PFE & 8) ? 2 : 0);
    if (PFE && hf == PFH) {
      const int fg0 = (14 * xc) >> 2, gvo = (lane >> 5) * (FF * 2) + (lane & 31) * 16;
      int n = 32 + 16 * PFH;
      if (PFE & 1) {
        const rsrc_t rsO = mk(a.wo, 4096 * 512 * 2);
#pragma unroll
        for (int i = 0; i < 4; ++i) { dma_df(rsO, lane * 16, (512 * xc + 16 * r + 4 * i + j) * 1024, L_OB + v * 2048); AWAIT(n, DEPTH); ++n; }
      }
      if (PFE & 8) {
        const rsrc_t rsG = mk(a.wg, 512 * FF * 2);
#pragma unroll
        for (int i = 0; i < 2; ++i) { dma_df(rsG, gvo, (16 * r + 8 * i + 2 * j) * (FF * 2) + 128 * fg0, L_OB + v * 2048); AWAIT(n, DEPTH); ++n; }
      }
      if (PFE & 4) {
        const rsrc_t rsU = mk(a.wu, 512 * FF * 2);
#pragma unroll
        for (int i = 0; i < 2; ++i) { dma_df(rsU, gvo, (16 * r + 8 * i + 2 * j) * (FF * 2) + 128 * fg0, L_OB + v * 2048); AWAIT(n, DEPTH); ++n; }
      }
      if (PFE & 2) {
        const rsrc_t rsD = mk(a.wd, FF * 512 * 2);
#pragma unroll
        for (int i = 0; i < 2; ++i) { dma_df(rsD, lane * 16, (64 * fg0 + 8 * r + 4 * i + j) * 1024, L_OB + v * 2048); AWAIT(n, DEPTH); ++n; }
      }
    }
#pragma unroll
    for (int i = 0; i < 16; ++i) { vr[i] = __builtin_amdgcn_raw_buffer_load_b128(rsV, lane * 16 + j * 1024, (hf * 16 + i) * 4096, NT); AWAIT(32 + (hf >= PFH ? NPF : 0) + hf * 16 + i, DEPTH); }
    SBAR();
    lsignal(hf ? F_VI : F_KI + j);
    __builtin_amdgcn_s_setprio(3);
    STAMP(1);
    unsigned s2[2][2];
#pragma unroll
    for (int hs = 0; hs < 2; ++hs)
#pragma unroll
      for (int d = 0; d < 2; ++d) {
        const unsigned b01 = hs ? 0x0302u : 0x0100u;
        s2[hs][d] = (d == ((lane >> 1) & 1)) ? ((lane & 1) ? ((b01 << 16) | 0x0c0cu) : (0x0c0c0000u | b01)) : 0x0c0c0c0cu;
      }
    lwait(L, hf ? F_V1 + j : F_L, hf ? 1 : 4);
    STAMP(2);
    const lptr pb = L + L_PB + (((j * 2 + hf) * 4 + G) * 4 + (i16 >> 2)) * 32;
    const v4u pw[2] = {*(LAS v4u*)pb, *(LAS v4u*)(pb + 16)};
    f32x4 acc[2] = {Z4, Z4};
    float fs[2][4];
    if (hf) {
      acc[0] = *(LAS f32x4*)(L + L_OB + j * 2048 + lane * 32); acc[1] = *(LAS f32x4*)(L + L_OB + j * 2048 + lane * 32 + 16);
#pragma unroll
      for (int q = 0; q < 2; ++q) {
        const LAS float* ms = (const LAS float*)(L + L_MST) + (2 * q + (lane >> 5)) * 2;
        const float M = fmaxf(fmaxf(ms[0], ms[8]), fmaxf(ms[16], ms[24]));
#pragma unroll
        for (int k = 0; k < 4; ++k) fs[q][k] = __builtin_amdgcn_exp2f((ms[k * 8] - M) * LOG2E);
      }
    }
    tag = __builtin_amdgcn_readfirstlane(lflag(L, F_TAG));
#pragma unroll
    for (int i = 0; i < 16; ++i) {
      const unsigned src = pw[i >> 3][(i >> 1) & 3];
      const v2u A = {__builtin_amdgcn_perm(src, src, s2[i & 1][0]), __builtin_amdgcn_perm(src, src, s2[i & 1][1])};
      acc[0] = mfma16(A, v2u{vr[i].x, vr[i].y}, acc[0]);
      acc[1] = mfma16(A, v2u{vr[i].z, vr[i].w}, acc[1]);
    }
    STAMP(3);
#pragma unroll
    for (int q = 0; q < 2; ++q) *(LAS f32x4*)(L + L_OB + v * 2048 + lane * 32 + q * 16) = acc[q];
    if (hf == 0) lsignal(F_V1 + j);
    else if (larrive(F_V) == 3u) {
      f32x4 p[2][4];
#pragma unroll
      for (int q = 0; q < 2; ++q)
#pragma unroll
        for (int k = 0; k < 4; ++k) p[q][k] = *(LAS f32x4*)(L + L_OB + (4 + k) * 2048 + (q * 64 + lane) * 16);
#pragma unroll
      for (int q = 0; q < 2; ++q)
        sth(rsW, WS_OP + (xc * 32 + r) * 2048 + (q * 64 + lane) * 16, enc<8>((p[q][0] * fs[q][0] + p[q][1] * fs[q][1]) + (p[q][2] * fs[q][2] + p[q][3] * fs[q][3]), tag));
      lsignal(F_PUB);
      STAMP(4);
    }
    if (!isC || ABL) EXIT();
    __builtin_amdgcn_s_setprio(0);
    lwait(L, F_PUB, 1);
    if (DNLAST) lwait(L, F_UP, 8);
    v4u dn[8];
    {
      const rsrc_t rsD = mk(a.wd, FF * 512 * 2);
#pragma unroll
      for (int i = 0; i < 8; ++i) dn[i] = __builtin_amdgcn_raw_buffer_load_b128(rsD, (lane >> 3) * 1024 + (lane & 7) * 16, (64 * fg + 8 * i) * 1024 + 128 * v, 0);
      SBAR();
    }
    v4u wf[2][4];
    if (DNLAST) { wg_barrier(); gu_load(L, wf, w, lane); }
    v2u dh[4][4];
#pragma unroll
    for (int c = 0; c < 4; ++c) {
      const lptr sc = L + L_OB + v * 2048;
      *(LAS v4u*)(sc + lane * 16) = dn[2 * c]; *(LAS v4u*)(sc + 1024 + lane * 16) = dn[2 * c + 1];
      CBAR();
#pragma unroll
      for (int t = 0; t < 4; ++t) dh[c][t] = __builtin_bit_cast(v2u, __builtin_amdgcn_ds_read_tr16_b64_v4i16((LAS s16x4*)(sc + (lane >> 2) * 128 + t * 32 + (lane & 3) * 8)));
      CBAR();
    }
    if (!DNLAST) { wg_barrier(); gu_load(L, wf, w, lane); }
    STAMP(7);
    wg_barrier();
    wg_barrier();
    gu_mma(L, wf, w, lane);
    wg_barrier();
    STAMP(8);
    {
      f32x2 pg[4], pu[4];
#pragma unroll
      for (int k = 0; k < 4; ++k) { pg[k] = *(LAS f32x2*)(L + L_GU + k * 1024 + lane * 8); pu[k] = *(LAS f32x2*)(L + L_GU + k * 1024 + 512 + lane * 8); }
      const f32x2 gt = rb2((pg[0] + pg[1]) + (pg[2] + pg[3])), up = rb2((pu[0] + pu[1]) + (pu[2] + pu[3]));
      const lptr ab = L + L_PB + v * 256;
      *(LAS unsigned*)(ab + lane * 4) = pk2(rb2(f32x2{silu_f(gt.x), silu_f(gt.y)}) * up);
      CBAR();
      v2u ah[4];
#pragma unroll
      for (int c = 0; c < 4; ++c) ah[c] = *(LAS v2u*)(ab + (lane & 1) * 128 + c * 32 + G * 8);
      f32x4 fa[4];
#pragma unroll
      for (int t = 0; t < 4; ++t) fa[t] = mfma(v4u{ah[0].x, ah[0].y, ah[1].x, ah[1].y}, v4u{dh[0][t].x, dh[0][t].y, dh[1][t].x, dh[1][t].y}, Z4);
#pragma unroll
      for (int t = 0; t < 4; ++t) fa[t] = mfma(v4u{ah[2].x, ah[2].y, ah[3].x, ah[3].y}, v4u{dh[2][t].x, dh[2][t].y, dh[3][t].x, dh[3][t].y}, fa[t]);
      const f32x2 f = sel(G & 2, sel(G & 1, lo2(fa[3]), lo2(fa[2])), sel(G & 1, lo2(fa[1]), lo2(fa[0])));
      const unsigned tg = (tag + (lane & 3)) & 0xffu;
      const int so = WS_FP + ((cb0 * 16 + 2 * v + (lane >> 5)) * 28 + fg) * 128 + (lane & 31) * 4;
      __builtin_amdgcn_raw_buffer_store_b32(((__float_as_uint(f.x) + 0x80u) & ~0xffu) | tg, rsW, so, 0, SC1);
      __builtin_amdgcn_raw_buffer_store_b32(((__float_as_uint(f.y) + 0x80u) & ~0xffu) | tg, rsW, so + 16 * 3584, 0, SC1);
    }
    STAMP(10);
    EXIT();
  } else if (w >= 4) {
    const int j = w - 4;
    const rsrc_t rsK = mk(a.kc + kvoff, 131072);
    unsigned voff[4];
#pragma unroll
    for (int q = 0; q < 4; ++q) voff[q] = j * 1024 + G * 256 + ((i16 ^ (4 * q + G)) << 4);
    const bool after = T_K ? early : CRITFIRST && er >= 16 && er < 20;
    if (after) { wg_barrier(); if (T_K) pause_until(tw0 + T_K); }
    FSTAMP(3, 7);
#pragma unroll
    for (int i = 0; i < INIT; ++i) dma_nt(rsK, voff[i & 3], i * 4096, i * 4096 + j * 1024);
    FSTAMP(3, 9);
    if (!after) wg_barrier();
    FSTAMP(3, 10);
    if (FINE == 3) { vmw<INIT - 1>(); FSTAMP(3, 12); }
#pragma unroll
    for (int i = INIT; i < 32; ++i) {
      if (i == 24 && early) lwait(L, F_EW, 4);
      PACE(i); dma_nt(rsK, voff[i & 3], i * 4096, i * 4096 + j * 1024);
      AWAIT(i, DEPTH);
    }
    lsignal(F_KI + j);
    STAMP(1);
#define KREAD(u) _Pragma("unroll") for (int ks = 0; ks < 4; ++ks) kf[u][ks] = *(LAS v4u*)(kb + (u) * 16384 + (((4 * ks + G) ^ i16) << 4))
    int real = DRY ? 0 : 1;
#pragma unroll 1
    for (;; real = 1) {
    int i16 = lane & 15, G = lane >> 4;
    asm volatile("" : "+s"(real), "+v"(i16), "+v"(G));
    const lptr kb = L + j * 1024 + (i16 >> 2) * 4096 + (i16 & 3) * 256;
    v4u kf[8][4];
    KREAD(0); KREAD(1); KREAD(2); KREAD(3);
    if (real) lsignal(F_KR);
    KREAD(4);
    SBAR();
    if (real) {
      vmw<0>();
      STAMP(2);
      lwait(L, F_QR, 4);
      STAMP(3);
    }
    const int h = i16 & 3, rho = i16 >> 2;
    v4u qf[4];
#pragma unroll
    for (int ks = 0; ks < 4; ++ks) qf[ks] = *(LAS v4u*)(L + L_QS + h * 256 + ks * 64 + G * 16);
    tag = lflag(L, F_TAG);
    SBAR();
    FSTAMP(1, 6);
    f32x4 sc[8];
#define KMMA2(u, v_) sc[u] = mfma(kf[u][0], qf[0], Z4); sc[v_] = mfma(kf[v_][0], qf[0], Z4); \
    _Pragma("unroll") for (int ks = 1; ks < 4; ++ks) { sc[u] = mfma(kf[u][ks], qf[ks], sc[u]); sc[v_] = mfma(kf[v_][ks], qf[ks], sc[v_]); }
    KMMA2(0, 1);
    FSTAMP(1, 7);
    SBAR();
    KREAD(5); KREAD(6);
    SBAR();
    KMMA2(2, 3);
    SBAR();
    KREAD(7);
    SBAR();
    FSTAMP(1, 8);
    KMMA2(4, 5); KMMA2(6, 7);
    FSTAMP(1, 9);
    tag = __builtin_amdgcn_readfirstlane(tag);
    f32x2 s[4];
#pragma unroll
    for (int aa = 0; aa < 2; ++aa) {
      const f32x4 t = sel(rho & 2, sel(rho & 1, sc[4 * aa + 3], sc[4 * aa + 2]), sel(rho & 1, sc[4 * aa + 1], sc[4 * aa]));
      s[2 * aa] = rb2(rb2(lo2(t)) * 0.08838834764831845f); s[2 * aa + 1] = rb2(rb2(hi2(t)) * 0.08838834764831845f);
    }
    FSTAMP(1, 10);
    float m = fmaxf(fmaxf(fmaxf(s[0].x, s[0].y), fmaxf(s[1].x, s[1].y)), fmaxf(fmaxf(s[2].x, s[2].y), fmaxf(s[3].x, s[3].y)));
    m = fmaxf(m, dpp<ROR4>(m)); m = fmaxf(m, dpp<ROR8>(m)); m = x32_max(x16_max(m));
    FSTAMP(1, 11);
    f32x2 ls = {0.f, 0.f};
    LAS unsigned short* pd = (LAS unsigned short*)(L + L_PB) + j * 512 + h * 16 + 4 * rho + G;
#pragma unroll
    for (int k = 0; k < 4; ++k) {
      const f32x2 e = (s[k] - m) * LOG2E;
      const f32x2 p = {__builtin_amdgcn_exp2f(e.x), __builtin_amdgcn_exp2f(e.y)};
      ls += p;
      const unsigned u = pk2(p);
      pd[k * 128] = (unsigned short)u; pd[k * 128 + 64] = (unsigned short)(u >> 16);
    }
    FSTAMP(1, 12);
    float l = ls.x + ls.y;
    l += dpp<ROR4>(l); l += dpp<ROR8>(l); l = x32_sum(x16_sum(l));
    if (lane < 4) *(LAS f32x2*)(L + L_MST + (j * 4 + lane) * 8) = f32x2{m, l};
    FSTAMP(1, 13);
    FSTAMP(1, 14);
    if (real) break;
    }
    if (larrive(F_L) == 3u) {
      if (lane < 4) {
        const LAS float* ms = (const LAS float*)(L + L_MST) + lane * 2;
        const float M = fmaxf(fmaxf(ms[0], ms[8]), fmaxf(ms[16], ms[24]));
        float Ls = 0.f;
#pragma unroll
        for (int k = 0; k < 4; ++k) Ls += ms[k * 8 + 1] * __builtin_amdgcn_exp2f((ms[k * 8] - M) * LOG2E);
        sth(rsW, WS_ST + ((xc * 32 + r) * 4 + lane) * 16, v4u{__float_as_uint(M), tag, __float_as_uint(Ls), ~tag});
      }
    }
    STAMP(4);
  } else {
    const int cw = w;
    const bool isQ = er < 16;
#define TAG_READY() do { if (cw == 0) { if (lane == 0) ((LAS unsigned*)(L + L_FLAGS))[F_TAG] = ep + 1u; lsignal(F_TR); } } while (0)
#define TAG_GET() do { lwait(L, F_TR, 1); tag = __builtin_amdgcn_readfirstlane(lflag(L, F_TAG)); } while (0)
    if (early) {
      const int kh = er & 1, cgq = er >> 1, krow = 256 * kh + 64 * cw;
      const int mg = cgq < 10 ? 2 * xc + (cgq - 8) : 16 + 4 * xc + (cgq - 10);
      const rsrc_t rsE = isQ ? mk(a.wq, 512 * 4096 * 2) : mk(a.wm, 512 * 3072 * 2), rsA = mk(isQ ? a.x : a.c, 8192);
      const int rowb = isQ ? 8192 : 6144, colb = isQ ? (512 * xc + 64 * cgq) * 2 : 128 * mg;
      v2u af[2];
      v4u xr[2];
      unsigned ep;
      asm volatile("s_nop 4\n\tbuffer_load_dwordx2 %0, %5, %7, 0 offen\n\tbuffer_load_dwordx2 %1, %5, %7, 0 offen offset:64\n\t"
                   "buffer_load_dwordx4 %2, %6, %7, 0 offen\n\tbuffer_load_dwordx4 %3, %6, %7, 0 offen offset:1024\n\tbuffer_load_dword %4, off, %8, %9 sc1"
                   : "=&v"(af[0]), "=&v"(af[1]), "=&v"(xr[0]), "=&v"(xr[1]), "=&v"(ep)
                   : "v"((lane & 7) * 1024 + (krow + 8 * G + (lane & 8) / 2) * 2), "v"(isQ ? 2 * cw * 1024 + lane * 16 : 8192), "s"(rsA), "s"(rsW), "s"(cw == 0 ? WS_EPOCH : WS_TOTAL) : "memory");
      const bool crit = !isQ && cgq < 10;
      if (crit) FSTAMP(2, 7);
      if (T_Q && !crit) { wg_barrier(); pause_until(tw0 + (isQ ? T_Q : T_M2)); }
#pragma unroll
      for (int i = 0; i < 8; ++i) dma_nt(rsE, (lane >> 3) * rowb + (lane & 7) * 16, (krow + 8 * i) * rowb + colb, L_EW + cw * 8192 + i * 1024);
      if (crit) FSTAMP(2, 8);
      if (!T_Q || crit) wg_barrier();
      if (crit) FSTAMP(2, 9);
      asm volatile("s_waitcnt vmcnt(8)" : "+v"(ep), "+v"(af[0]), "+v"(af[1]), "+v"(xr[0]), "+v"(xr[1])::"memory");
      if (crit) FSTAMP(2, 10);
      TAG_READY();
      v4u bf[2];
      if (!isQ) {
#pragma unroll
        for (int ks = 0; ks < 2; ++ks) {
          const unsigned s0 = pk2(silu_f(bflo(af[ks].x)), silu_f(bfhi(af[ks].x))), s1 = pk2(silu_f(bflo(af[ks].y)), silu_f(bfhi(af[ks].y)));
          bf[ks] = v4u{s0, s1, dppu<ROR8>(s0), dppu<ROR8>(s1)};
        }
      } else {
#pragma unroll
        for (int i = 0; i < 2; ++i) {
          float e[8];
#pragma unroll
          for (int d = 0; d < 4; ++d) { e[2 * d] = bflo(xr[i][d]); e[2 * d + 1] = bfhi(xr[i][d]); }
          const float mean = wave_sum(((e[0] + e[1]) + (e[2] + e[3])) + ((e[4] + e[5]) + (e[6] + e[7]))) * (1.f / 512.f);
          float q = 0.f;
#pragma unroll
          for (int d = 0; d < 8; ++d) q += (e[d] - mean) * (e[d] - mean);
          const float var = wave_sum(q) * (1.f / 512.f);
          if (lane == 0) *(LAS f32x2*)(L + L_STAT + (2 * cw + i) * 8) = f32x2{rb(mean), rb(__builtin_amdgcn_sqrtf(rb(rb(var) + 1e-5f)))};
        }
        lsignal(F_LN);
        lwait(L, F_LN, 4);
        const f32x2 st = *(LAS f32x2*)(L + L_STAT + (lane & 7) * 8);
        const float rd = __builtin_amdgcn_rcpf(st.y);
#pragma unroll
        for (int ks = 0; ks < 2; ++ks) { af[ks].x = pk2(rb2(unpk(af[ks].x) - st.x) * rd); af[ks].y = pk2(rb2(unpk(af[ks].y) - st.x) * rd); }
      }
      TAG_GET();
      const int rd = crit ? (lane & 7) * 16 + (lane >> 3) : lane, rd2 = crit ? 8 : 64;
      const int st = crit ? WS_MC + 2 * (mg & 7) * 4096 + kh * 2048 + (mg >> 3) * 1024 + lane * 16
                   : isQ ? WS_QP + ((kh * 8 + G) * 4096 + 512 * xc + 64 * cgq + 4 * i16) * 4 : WS_MOD + ((kh * 8 + G) * 2048 + 64 * (mg - 16) + 4 * i16) * 4;
      if (crit) FSTAMP(2, 12);
      SBAR();
      vmw<0>();
      STAMP(1);
      if (crit) FSTAMP(2, 13);
      v4u wf[2][4];
#pragma unroll
      for (int ks = 0; ks < 2; ++ks)
#pragma unroll
        for (int t = 0; t < 4; ++t) wf[ks][t] = trfrag(L + L_EW + cw * 8192 + (32 * ks + 8 * G + (i16 >> 2)) * 128 + (i16 & 3) * 8 + t * 32);
      lsignal(F_EW);
      if (isQ) {
        v4u g[8];
        pause_until(tw0 + T_H1);
        if (!(ABL & 2)) gather<4, 8, 8, 1024>(rsW, WS_MC + (8 * kh + 2 * cw) * 4096 + lane * 16, g, tag);
        STAMP(3);
#pragma unroll
        for (int ks = 0; ks < 2; ++ks) {
          const f32x4 sa = asf(g[4 * ks]) + asf(g[4 * ks + 2]), ca = asf(g[4 * ks + 1]) + asf(g[4 * ks + 3]);
          const unsigned h0 = pk2(rb2(unpk(af[ks].x) * rb2(rb2(lo2(ca)) + 1.f)) + rb2(lo2(sa))), h1 = pk2(rb2(unpk(af[ks].y) * rb2(rb2(hi2(ca)) + 1.f)) + rb2(hi2(sa)));
          bf[ks] = v4u{h0, h1, dppu<ROR8>(h0), dppu<ROR8>(h1)};
        }
      }
      gemv_unit(L, rsW, wf, bf, cw, lane, rd, rd2, st, crit ? 4096 : isQ ? 4 * 4096 * 4 : 4 * 2048 * 4, false, 0u, tag);
      if (isQ) STAMP(4); else if (crit) STAMP(2);
    } else {
      const unsigned ep = __builtin_amdgcn_raw_buffer_load_b32(rsW, cw == 0 ? WS_EPOCH : WS_TOTAL, 0, SC1);
      wg_barrier();
      TAG_READY();
      TAG_GET();
    }
    lsignal(F_ED);
    if (cw < 2) {
      v4u g[2];
      pause_until(tw0 + T_H2);
      if (!(ABL & 2)) gather<4, 2, 8, 8 * 4096 * 4>(rsW, WS_QP + ((r >> 2) * 4096 + 512 * xc + 256 * cw + 4 * lane) * 4, g, tag);
      const f32x4 q = asf(g[0]) + asf(g[1]);
      *(LAS v2u*)(L + L_QS + cw * 512 + lane * 8) = v2u{pk2(lo2(q)), pk2(hi2(q))};
      STAMP(5);
    } else if (PRER) {
      const int c = cw - 2;
      if (PRER & 1) dma_df(rsW, lane * 16, WS_OP + (xc * 32 + r) * 2048 + c * 1024, L_PB + c * 1024);
      if (PRER & 2) { dma_df(rsW, lane * 16, WS_AP + r * 4096 + c * 2048, L_PB + c * 1024); dma_df(rsW, lane * 16, WS_AP + r * 4096 + c * 2048 + 1024, L_PB + c * 1024); }
      if ((PRER & 8) && r < 8) dma_df(rsW, lane * 16, WS_AP + (xc * 16 + r * 2 + c) * 1024, L_PB + c * 1024);
      if (PRER & 4) dma_df(rsW, lane < 56 ? lane * 16 : WS_TOTAL, WS_FP + xc * 16 * 3584 + (2 * r + c) * 896, L_PB + c * 1024);
      vmw<0>();
    }
    lsignal(F_QR);
  }

  if (ABL) EXIT();
  const int tw = w;
  const int gv0 = (lane >> 3) * (FF * 2) + (((lane & 7) ^ ((lane >> 3) & 2)) * 16), gv1 = gv0 ^ 64;
#define LATE_PF(dst) do { const int fg0_ = (14 * xc) >> 2, rr_ = 2 * (r - 16), gvo_ = (lane >> 5) * (FF * 2) + (lane & 31) * 16, so_ = (16 * rr_ + 4 * tw) * (FF * 2) + 128 * fg0_; \
      if (!(PFE & 8)) { const rsrc_t rsG_ = mk(a.wg, 512 * FF * 2); dma_df(rsG_, gvo_, so_, dst); dma_df(rsG_, gvo_, so_ + 2 * (FF * 2), dst); } \
      if (!(PFE & 4)) { const rsrc_t rsU_ = mk(a.wu, 512 * FF * 2); dma_df(rsU_, gvo_, so_, dst); dma_df(rsU_, gvo_, so_ + 2 * (FF * 2), dst); } \
      if (!(PFE & 2) && LPD) { const rsrc_t rsD_ = mk(a.wd, FF * 512 * 2); const int sd_ = (64 * fg0_ + 8 * rr_ + 2 * tw) * 1024; dma_df(rsD_, lane * 16, sd_, dst); dma_df(rsD_, lane * 16, sd_ + 1024, dst); } } while (0)
  if (r < 16) {
    const int cg = r & 7, bg = r >> 3;
    lwait(L, F_KR, 4);
    lwait(L, F_ED, 4);
    lwait(L, F_VI, 4);
    {
      const rsrc_t rsO = mk(a.wo, 4096 * 512 * 2);
#pragma unroll
      for (int i = 0; i < 8; ++i) { const int p = i ^ (4 * bg); dma_df(rsO, (lane >> 3) * 1024 + (lane & 7) * 16, (512 * xc + 64 * tw + 8 * p) * 1024 + 128 * cg, tw * 8192 + p * 1024); vmw<DEPTHW - 1>(); }
    }
    STAMP(14);
    lwait(L, F_PUB, 1);
    if (T_BP) pause_until(now() + T_BP);
    STAMP(8);
    {
      v4u g[4];
      const int o0 = WS_OP + (xc * 32 + (4 * bg + G) * 4) * 2048 + tw * 256 + i16 * 16;
      v4u gs;
      const int os = WS_ST + ((xc * 32 + (4 * bg + G) * 4 + (lane & 3)) * 4 + (tw >> 1)) * 16;
      gs = ldh(rsW, os);
#pragma unroll
      for (int k = 0; k < 4; ++k) g[k] = __builtin_amdgcn_raw_buffer_load_b128(rsW, o0, k * 2048, SC1);
      SBAR();
#pragma unroll 1
      for (unsigned i = 0; i < GSPIN; ++i) {
        if (__all(gs.y == tag && gs.w == ~tag)) break;
        CBAR();
        gs = ldh(rsW, os);
      }
      CBAR();
      v4u wf[2][4];
#pragma unroll
      for (int kk = 0; kk < 2; ++kk)
#pragma unroll
        for (int t = 0; t < 4; ++t) wf[kk][t] = trfrag(L + tw * 8192 + (8 * G + (i16 >> 2)) * 128 + (i16 & 3) * 8 + kk * 4096 + t * 32);
      float scl[4];
      {
        const float ms = __uint_as_float(gs.x), ls = __uint_as_float(gs.z);
        float M = fmaxf(ms, dpp<XOR1>(ms)); M = fmaxf(M, dpp<XOR2>(M));
        const float e = __builtin_amdgcn_exp2f((ms - M) * LOG2E);
        float den = e * ls; den += dpp<XOR1>(den); den += dpp<XOR2>(den);
        const float sc = e * __builtin_amdgcn_rcpf(den);
        scl[0] = dpp<0x00>(sc); scl[1] = dpp<0x55>(sc); scl[2] = dpp<0xAA>(sc); scl[3] = dpp<0xFF>(sc);
      }
      STAMP(7);
      {
        const v4u tv = tagv<8>(tag);
        unsigned bad = 0;
#pragma unroll
        for (int k = 0; k < 4; ++k) { g[k] ^= tv; bad |= g[k].x | g[k].y | g[k].z | g[k].w; }
        if (!__all((bad & 0xffu) == 0u)) gather<8, 4, PSLEEP, 2048>(rsW, o0, g, tag);
      }
      STAMP(9);
      const f32x4 o = (asf(g[0]) * scl[0] + asf(g[1]) * scl[1]) + (asf(g[2]) * scl[2] + asf(g[3]) * scl[3]);
      const lptr ob = L + L_PB + tw * 512;
      *(LAS v2u*)(ob + lane * 8) = v2u{pk2(lo2(o)), pk2(hi2(o))};
      CBAR();
      const v4u of0 = *(LAS v4u*)(ob + (lane & 3) * 128 + G * 16), of1 = *(LAS v4u*)(ob + (lane & 3) * 128 + 64 + G * 16);
      f32x4 acc[4];
#pragma unroll
      for (int t = 0; t < 4; ++t) acc[t] = mfma(wf[0][t], of0, Z4);
#pragma unroll
      for (int t = 0; t < 4; ++t) acc[t] = mfma(wf[1][t], of1, acc[t]);
      if (i16 < 4) {
#pragma unroll
        for (int t = 0; t < 4; ++t) *(LAS f32x4*)(L + L_RED + tw * 1024 + i16 * 256 + t * 64 + G * 16) = acc[t];
      }
    }
    wg_barrier();
    if (tw == 0) {
      f32x4 p[8];
#pragma unroll
      for (int k = 0; k < 8; ++k) p[k] = *(LAS f32x4*)(L + L_RED + k * 1024 + lane * 16);
      sth(rsW, WS_AP + (((4 * bg + G) * 8 + xc) * 512 + 64 * cg + 4 * i16) * 4, enc<8>(((p[0] + p[1]) + (p[2] + p[3])) + ((p[4] + p[5]) + (p[6] + p[7])), tag));
      STAMP(10);
    }
    if (tw != 1) EXIT();
    const int col = 32 * r + 4 * (lane & 7), pg = lane >> 3;
    const v2u xx = __builtin_amdgcn_raw_buffer_load_b64(mk(a.x, 8192), (xc * 512 + col) * 2, 0, 0);
    f32x4 ga4, gb4;
    {
      v4u m0[2], m1[2];
      gather<4, 2, 2, 65536>(rsW, WS_MOD + (xc * 2048 + col) * 4, m0, tag);
      gather<4, 2, 2, 65536>(rsW, WS_MOD + (xc * 2048 + 1536 + col) * 4, m1, tag);
      ga4 = asf(m0[0]) + asf(m0[1]); gb4 = asf(m1[0]) + asf(m1[1]);
    }
    const int e = (lane >> 5) * 2 + ((lane >> 4) & 1);
    float x2, gbv;
    {
      v4u p[1];
      gather<8, 1, 2, 0>(rsW, WS_AP + ((xc * 8 + pg) * 512 + col) * 4, p, tag);
      f32x4 s = asf(p[0]);
#pragma unroll
      for (int d = 0; d < 4; ++d) s[d] += dpp<ROR8>(s[d]);
      const float av = sw32(sw16(s.x, s.y), sw16(s.z, s.w));
      const float gav = rb(e == 0 ? ga4.x : e == 1 ? ga4.y : e == 2 ? ga4.z : ga4.w);
      gbv = rb(e == 0 ? gb4.x : e == 1 ? gb4.y : e == 2 ? gb4.z : gb4.w);
      const unsigned xw = (e & 2) ? xx.y : xx.x;
      x2 = rb(((e & 1) ? bfhi(xw) : bflo(xw)) + rb(gav * rb(av)));
    }
    STAMP(11);
    {
      v4u g[4];
      const int o0 = WS_FP + (xc * 16 + r) * 3584 + lane * 16;
      gather<8, 4, 0, 1024>(rsW, o0, lane < 32 ? o0 : o0 - 512, g, tag);
      STAMP(12);
      const f32x4 g3 = lane < 32 ? asf(g[3]) : Z4;
      const f32x4 s = (asf(g[0]) + asf(g[1])) + (asf(g[2]) + g3);
      float f = sw32(sw16(s.x, s.y), sw16(s.z, s.w));
      f += dpp<ROR8>(f);
      const unsigned o = pk2(x2 + rb(gbv * rb(f)), 0.f);
      if ((lane & 8) == 0) __builtin_amdgcn_raw_buffer_store_b16((unsigned short)o, mk(a.out, 8192), (xc * 512 + col + e) * 2, 0, 0);
    }
    STAMP(13);
  } else {
    if (r >= 30) {
      lwait(L, F_L, 4);
      lwait(L, F_VI, 4);
      if (PF3) {
        const rsrc_t rsO = mk(a.wo, 4096 * 512 * 2);
#pragma unroll
        for (int i = 0; i < 4; ++i) dma_df(rsO, (lane >> 3) * 1024 + (lane & 7) * 16, (512 * xc + 64 * tw + 8 * (2 * i + (r & 1))) * 1024 + 128 * 3, tw * 8192);
      }
      LATE_PF(65536 + tw * 8192);
      if (wg == 240 && tw == 0) {
        v4u g[1];
        gather<8, 1, 16, 0>(rsW, WS_AP + (((lane >> 5) * 32 + ((lane >> 2) & 7)) * 512 + 4 * (lane & 3)) * 4, g, tag);
        if (lane == 0) __builtin_amdgcn_raw_buffer_store_b32(tag, rsW, WS_EPOCH, 0, SC1);
      }
      vmw<0>();
      EXIT();
    }
    const int b = cb0 + (tw >> 2), col = 128 * (tw & 3) + 4 * (lane & 31);
    const f32x2 xv = unpk(__builtin_amdgcn_raw_buffer_load_b32(mk(a.x, 8192), (b * 512 + col) * 2 + (lane >> 5) * 4, 0, 0));
    f32x2 ga, sb, cb1;
    {
      v4u md[3];
      gather<4, 3, 2, 2048>(rsW, WS_MOD + (((lane >> 5) * 8 + b) * 2048 + col) * 4, md, tag);
      f32x2 mv[3];
#pragma unroll
      for (int k = 0; k < 3; ++k) { const f32x4 t = asf(md[k]); mv[k] = rb2(f32x2{sw32(t.x, t.z), sw32(t.y, t.w)}); }
      ga = mv[0]; sb = mv[1]; cb1 = rb2(mv[2] + 1.f);
    }
    {
      const rsrc_t rsU = mk(a.wu, 512 * FF * 2), rsG = mk(a.wg, 512 * FF * 2);
      lwait(L, F_L, 4);
      lwait(L, F_VI, 4);
      LATE_PF(65536 + tw * 8192);
      if ((PFE & 12) != 12) vmw<((PFE & 2) || !LPD ? 0 : 2)>();
      if (CWAIT) lwait(L, F_PUB, 1);
#pragma unroll
      for (int i = 0; i < 8; ++i) dma_df(rsG, (i & 1) ? gv1 : gv0, (64 * tw + 8 * i) * (FF * 2) + 128 * fg, tw * 8192 + i * 1024);
#pragma unroll
      for (int i = 0; i < 8; ++i) dma_df(rsU, (i & 1) ? gv1 : gv0, (64 * tw + 8 * i) * (FF * 2) + 128 * fg, 65536 + tw * 8192 + i * 1024);
      lsignal(F_UP);
    }
    STAMP(11);
    vmw<0>();
    STAMP(12);
    wg_barrier();
    v4u wf[2][4];
    gu_load(L, wf, tw, lane);
    STAMP(7);
    f32x2 x2;
    {
      v4u g[4];
      const int o0 = WS_AP + ((b * 8 + 4 * (lane >> 5)) * 512 + col) * 4;
      gather<8, 4, PSLEEP, 2048>(rsW, o0, g, tag);
      STAMP(9);
      const f32x4 s = (asf(g[0]) + asf(g[1])) + (asf(g[2]) + asf(g[3]));
      x2 = rb2(xv + rb2(ga * rb2(f32x2{sw32(s.x, s.z), sw32(s.y, s.w)})));
    }
    {
      const float c0 = __builtin_bit_cast(float, __builtin_amdgcn_readfirstlane(__builtin_bit_cast(int, x2.x)));
      const f32x2 d = x2 - c0, dd = d * d;
      const float t = x16_sum(row_sum(sw32(d.x + d.y, dd.x + dd.y)));
      if ((lane & 31) == 0) *(LAS f32x2*)(L + L_STAT + tw * 16 + (lane >> 5) * 8) = f32x2{t, c0};
    }
    wg_barrier();
    {
      f32x4 st[4];
#pragma unroll
      for (int k = 0; k < 4; ++k) st[k] = *(LAS f32x4*)(L + L_STAT + (tw >> 2) * 64 + k * 16);
      float mw[4], qw = 0.f;
#pragma unroll
      for (int k = 0; k < 4; ++k) { const float u = st[k].x * (1.f / 128.f); mw[k] = st[k].y + u; qw += st[k].z - st[k].x * u; }
      const float mean = 0.25f * ((mw[0] + mw[1]) + (mw[2] + mw[3]));
      float e2 = 0.f;
#pragma unroll
      for (int k = 0; k < 4; ++k) e2 += (mw[k] - mean) * (mw[k] - mean);
      const f32x2 mv = rb2(f32x2{mean, fmaxf((qw + 128.f * e2) * (1.f / 512.f), 0.f)});
      const float rd = __builtin_amdgcn_rcpf(rb(__builtin_amdgcn_sqrtf(rb(mv.y + 1e-5f))));
      *(LAS unsigned*)(L + L_RED + (tw >> 2) * 1024 + col * 2 + (lane >> 5) * 4) = pk2(rb2(rb2(rb2(x2 - mv.x) * rd) * cb1) + sb);
    }
    wg_barrier();
    gu_mma(L, wf, tw, lane);
    wg_barrier();
  }
  EXIT();
}

extern "C" void kernel_launch(void* const* d_in, const int* in_sizes, int n_in, void* d_out, int out_size, void* d_ws, size_t ws_size, hipStream_t stream) {
  Args a;
  a.x = (const unsigned short*)d_in[0]; a.kc = (const unsigned short*)d_in[1]; a.vc = (const unsigned short*)d_in[2];
  a.wq = (const unsigned short*)d_in[3]; a.wo = (const unsigned short*)d_in[4];
  a.wg = (const unsigned short*)d_in[6]; a.wu = (const unsigned short*)d_in[7]; a.wd = (const unsigned short*)d_in[8];
  a.c = (const unsigned short*)d_in[9]; a.wm = (const unsigned short*)d_in[10];
  a.out = (unsigned short*)d_out; a.ws = (unsigned char*)d_ws;
  (void)hipFuncSetAttribute((const void*)decode_block, hipFuncAttributeMaxDynamicSharedMemorySize, L_TOTAL);
  hipLaunchKernelGGL(decode_block, dim3(256), dim3(1024), L_TOTAL, stream, a);
}
```
